```python
import torch

def _softmax(x, axis=-1):
    return torch.softmax(x, dim=axis)

def _gelu(x):
    return torch.nn.functional.gelu(x, approximate='tanh')

def _silu(x):
    return torch.nn.functional.silu(x)

def _sigmoid(x):
    return torch.sigmoid(x)

def _layernorm(x, axis=-1, eps=1e-05):
    m = x.mean(dim=axis, keepdim=True)
    v = x.var(dim=axis, keepdim=True, unbiased=False)
    return (x - m) / torch.sqrt(v + eps)

def _scatter_add(tgt, idx, src, axis=0):
    return tgt.clone().index_add_(axis, idx, src)

def _segment_sum(src, seg, n):
    out = torch.zeros((n,) + src.shape[1:], dtype=src.dtype)
    return out.index_add_(0, seg, src)

def _pad(x, pw):
    flat = tuple((v for pair in reversed(pw) for v in pair))
    return torch.nn.functional.pad(x, flat)
kernel_name = 'gather_scatter__tensor_product_equiv_equiformer__graph'

def setup_inputs():

    _w = lambda a: (lambda t: t.to(torch.bfloat16) if t.is_floating_point() else t)(torch.as_tensor(a))
    N, deg, C, De = (12500, 64, 64, max(0, 1))
    E = N * deg
    src = (lambda lo, hi, *s: torch.randint(lo, hi, s))(0, N, E).to(torch.int64)
    dst = (lambda lo, hi, *s: torch.randint(lo, hi, s))(0, N, E).to(torch.int64)
    rvec = (lambda *s: torch.randn(*s, dtype=torch.bfloat16) * 0.05)(E, 3).to(float)
    rn = (torch.linalg.norm(rvec, dim=-1, keepdim=True) + 1e-06).to(float)
    rhat = (rvec / rn).to(float)
    eps = torch.zeros((3, 3, 3), dtype=float)
    eps[0, 1, 2] = eps[1, 2, 0] = eps[2, 0, 1] = 1.0
    eps[0, 2, 1] = eps[2, 1, 0] = eps[1, 0, 2] = -1.0
    return {'x': _w((lambda *s: torch.randn(*s, dtype=torch.bfloat16) * 0.4)(N, 4 * C).to(float)), 'rhat': _w(rhat), 'e_attr': _w((lambda *s: torch.randn(*s, dtype=torch.bfloat16) * 0.4)(E, De).to(float)), 'src': _w(src), 'dst': _w(dst), 'w_rad': _w((lambda *s: torch.randn(*s, dtype=torch.bfloat16) * 0.4)(De, 5 * C).to(float)), 'w_si0': _w((lambda *s: torch.randn(*s, dtype=torch.bfloat16) * 0.4)(2 * C, C).to(float)), 'w_si1': _w((lambda *s: torch.randn(*s, dtype=torch.bfloat16) * 0.05)(3 * C, C).to(float)), 'cg_eps': _w(eps)}

def reference(x, rhat, e_attr, src, dst, w_rad, w_si0, w_si1, cg_eps):
    N = x.shape[0]
    C = w_si0.shape[1]
    E = rhat.shape[0]
    x0 = x[:, :C].clone()
    x1 = x[:, C:].reshape(N, 3, C)
    R = (e_attr @ w_rad).reshape(E, 5, C)
    f0 = x0[src]
    f1 = x1[src]
    Y1 = rhat
    p00_0 = f0 * R[:, 0, :]
    p11_0 = torch.einsum('ejc,ej->ec', f1, Y1) * R[:, 1, :]
    p01_1 = torch.einsum('ec,ej->ejc', f0, Y1) * R[:, 2, :][:, None, :]
    p10_1 = f1 * R[:, 3, :][:, None, :]
    p11_1 = torch.einsum('ijk,ekc,ej->eic', cg_eps, f1, Y1) * R[:, 4, :][:, None, :]
    m0 = torch.concatenate([p00_0, p11_0], axis=-1) @ w_si0
    m1 = torch.einsum('ejp,pc->ejc', torch.concatenate([p01_1, p10_1, p11_1], axis=-1), w_si1)
    agg0 = _segment_sum(m0, dst, N)
    agg1 = _segment_sum(m1.reshape(E, 3 * C), dst, N).reshape(N, 3, C)
    gate = _sigmoid(agg0)
    out1 = agg1 * gate[:, None, :]
    return torch.concatenate([agg0, out1.reshape(N, 3 * C)], axis=-1)
```

```cpp
#include <hip/hip_runtime.h>
#include <hip/hip_bf16.h>
#include <stdint.h>

typedef unsigned short u16;
__device__ __forceinline__ float bf2f(u16 v) { return __uint_as_float(((unsigned)v) << 16); }
__device__ __forceinline__ u16 f2bf(float f) { __hip_bfloat16 b = __float2bfloat16(f); return *(u16*)&b; }

__global__ void k_zero(int* p, int n) { int i = blockIdx.x * blockDim.x + threadIdx.x; if (i < n) p[i] = 0; }
__global__ void k_hist(const int* __restrict__ dst, int E, int* cnt) {
  int e = blockIdx.x * blockDim.x + threadIdx.x; if (e < E) atomicAdd(&cnt[dst[e]], 1); }
__global__ void k_scan(const int* __restrict__ cnt, int N, int* off, int* cur) {
  __shared__ int s[1024];
  int t = threadIdx.x; int per = (N + 1023) / 1024; int b = t * per; int sum = 0;
  for (int i = 0; i < per; ++i) if (b + i < N) sum += cnt[b + i];
  s[t] = sum; __syncthreads();
  for (int d = 1; d < 1024; d <<= 1) { int v = (t >= d) ? s[t - d] : 0; __syncthreads(); s[t] += v; __syncthreads(); }
  int run = s[t] - sum;
  for (int i = 0; i < per; ++i) if (b + i < N) { off[b + i] = run; cur[b + i] = run; run += cnt[b + i]; }
  if (t == 1023) off[N] = s[1023];
}
__global__ void k_fill(const int* __restrict__ dst, int E, int* cur, int* list) {
  int e = blockIdx.x * blockDim.x + threadIdx.x; if (e < E) { int p = atomicAdd(&cur[dst[e]], 1); list[p] = e; } }

__global__ __launch_bounds__(64) void k_agg(const u16* __restrict__ x, const u16* __restrict__ rhat, const u16* __restrict__ ea,
    const int* __restrict__ src, const int* __restrict__ off, const int* __restrict__ list,
    const u16* __restrict__ wrad, const u16* __restrict__ w0, const u16* __restrict__ w1, const u16* __restrict__ eps,
    u16* __restrict__ out) {
  __shared__ float X0[128]; __shared__ float X1[3][192];
  int n = blockIdx.x, c = threadIdx.x;
  float T[4][4];
  for (int q = 0; q < 4; ++q) for (int k = 0; k < 4; ++k) T[q][k] = 0.f;
  for (int i = off[n]; i < off[n + 1]; ++i) {
    int e = list[i]; int s = src[e]; float a = bf2f(ea[e]);
    float cf[4] = {a, a * bf2f(rhat[3 * e]), a * bf2f(rhat[3 * e + 1]), a * bf2f(rhat[3 * e + 2])};
    float f[4]; for (int k = 0; k < 4; ++k) f[k] = bf2f(x[(size_t)s * 256 + k * 64 + c]);
    for (int q = 0; q < 4; ++q) for (int k = 0; k < 4; ++k) T[q][k] += cf[q] * f[k];
  }
  float wr[5]; for (int k = 0; k < 5; ++k) wr[k] = bf2f(wrad[k * 64 + c]);
  X0[c] = T[0][0] * wr[0];
  X0[64 + c] = (T[1][1] + T[2][2] + T[3][3]) * wr[1];
  for (int i = 0; i < 3; ++i) {
    X1[i][c] = T[1 + i][0] * wr[2];
    X1[i][64 + c] = T[0][1 + i] * wr[3];
    float s = 0.f;
    for (int j = 0; j < 3; ++j) for (int k = 0; k < 3; ++k) s += bf2f(eps[i * 9 + j * 3 + k]) * T[1 + j][1 + k];
    X1[i][128 + c] = s * wr[4];
  }
  __syncthreads();
  float a0 = 0.f; for (int k = 0; k < 128; ++k) a0 += X0[k] * bf2f(w0[k * 64 + c]);
  float a1[3] = {0.f, 0.f, 0.f};
  for (int k = 0; k < 192; ++k) { float w = bf2f(w1[k * 64 + c]); a1[0] += X1[0][k] * w; a1[1] += X1[1][k] * w; a1[2] += X1[2][k] * w; }
  float g = 1.f / (1.f + __expf(-a0));
  out[(size_t)n * 256 + c] = f2bf(a0);
  for (int i = 0; i < 3; ++i) out[(size_t)n * 256 + 64 + i * 64 + c] = f2bf(a1[i] * g);
}

extern "C" void kernel_launch(void* const* d_in, const int* in_sizes, int n_in, void* d_out, int out_size, void* d_ws, size_t ws_size, hipStream_t stream) {
  const u16* x = (const u16*)d_in[0]; const u16* rhat = (const u16*)d_in[1]; const u16* ea = (const u16*)d_in[2];
  const int* src = (const int*)d_in[3]; const int* dst = (const int*)d_in[4];
  const u16* wrad = (const u16*)d_in[5]; const u16* w0 = (const u16*)d_in[6]; const u16* w1 = (const u16*)d_in[7]; const u16* eps = (const u16*)d_in[8];
  int N = in_sizes[0] / 256; int E = in_sizes[3];
  int* cnt = (int*)d_ws; int* off = cnt + 16384; int* cur = off + 16384; int* list = cur + 16384;
  k_zero<<<(N + 255) / 256, 256, 0, stream>>>(cnt, N);
  k_hist<<<(E + 255) / 256, 256, 0, stream>>>(dst, E, cnt);
  k_scan<<<1, 1024, 0, stream>>>(cnt, N, off, cur);
  k_fill<<<(E + 255) / 256, 256, 0, stream>>>(dst, E, cur, list);
  k_agg<<<N, 64, 0, stream>>>(x, rhat, ea, src, off, list, wrad, w0, w1, eps, (u16*)d_out);
}
```

```cpp
#include <hip/hip_runtime.h>
#include <stdint.h>

typedef unsigned short u16;
typedef unsigned u32;
typedef __attribute__((ext_vector_type(4))) unsigned u32x4;
typedef __attribute__((ext_vector_type(2))) unsigned u32x2;
typedef __attribute__((ext_vector_type(4))) float f32x4;
typedef __attribute__((ext_vector_type(16))) float f32x16;
typedef __attribute__((ext_vector_type(8))) __bf16 bf16x8;
typedef __attribute__((ext_vector_type(4))) short s16x4;
typedef __attribute__((ext_vector_type(2))) __bf16 bf16x2;
#define LDSP(T) __attribute__((address_space(3))) T*

constexpr int NB = 49;
constexpr int NBK = 256;
constexpr int PA = 256;
constexpr int CHMAX = 3136;
constexpr int CAP = 4096;
constexpr int NR = 16;

__device__ __forceinline__ float bf2f(u32 v) { return __uint_as_float(v << 16); }
__device__ __forceinline__ u32 pk2(float lo, float hi) {
  bf16x2 v; v[0] = (__bf16)lo; v[1] = (__bf16)hi; return __builtin_bit_cast(u32, v);
}
template <int CTRL> __device__ __forceinline__ float dppf(float v) {
  return __builtin_bit_cast(float, __builtin_amdgcn_mov_dpp(__builtin_bit_cast(int, v), CTRL, 0xf, 0xf, true));
}
__device__ __forceinline__ u32 wave_incl_scan(u32 v) {
  v += (u32)__builtin_amdgcn_update_dpp(0, (int)v, 0x111, 0xf, 0xf, true);
  v += (u32)__builtin_amdgcn_update_dpp(0, (int)v, 0x112, 0xf, 0xf, true);
  v += (u32)__builtin_amdgcn_update_dpp(0, (int)v, 0x114, 0xf, 0xf, true);
  v += (u32)__builtin_amdgcn_update_dpp(0, (int)v, 0x118, 0xf, 0xf, true);
  v += (u32)__builtin_amdgcn_update_dpp(0, (int)v, 0x142, 0xa, 0xf, false);
  v += (u32)__builtin_amdgcn_update_dpp(0, (int)v, 0x143, 0xc, 0xf, false);
  return v;
}

__global__ __launch_bounds__(1024) void k_part(const int* __restrict__ src, const int* __restrict__ dst, const u16* __restrict__ rhat,
                                               const u16* __restrict__ ea, const u16* __restrict__ wrad, const u16* __restrict__ w0,
                                               const u16* __restrict__ w1, int E, int CH, u32* __restrict__ rec, u32* __restrict__ offs,
                                               u32* __restrict__ wfrag) {
  __shared__ u32 cnt[16 * NBK];
  __shared__ u32 skey[CHMAX];
  __shared__ u32x2 spay[CHMAX];
  __shared__ u32 sbase[NBK];
  __shared__ u32 wtot[4];
  const int t = threadIdx.x, p = blockIdx.x, wv = t >> 6, lane = t & 63;
  const int e0 = p * CH + 4 * t;
  const int nval = min(CH, E - p * CH);
  const int nmine = min(4, nval - 4 * t);
  u32 d[4], s[4], a[4], y[4][3];
  if (nmine == 4) {
    const u32x4 dv = *(const u32x4*)(dst + e0), sv = *(const u32x4*)(src + e0);
    const u32x2 av = *(const u32x2*)(ea + e0);
    const u32x2 r0 = *(const u32x2*)(rhat + 3 * (size_t)e0), r1 = *(const u32x2*)(rhat + 3 * (size_t)e0 + 4), r2 = *(const u32x2*)(rhat + 3 * (size_t)e0 + 8);
    for (int i = 0; i < 4; ++i) { d[i] = dv[i]; s[i] = sv[i]; }
    a[0] = av[0] & 0xffff; a[1] = av[0] >> 16; a[2] = av[1] & 0xffff; a[3] = av[1] >> 16;
    y[0][0] = r0[0] & 0xffff; y[0][1] = r0[0] >> 16; y[0][2] = r0[1] & 0xffff;
    y[1][0] = r0[1] >> 16; y[1][1] = r1[0] & 0xffff; y[1][2] = r1[0] >> 16;
    y[2][0] = r1[1] & 0xffff; y[2][1] = r1[1] >> 16; y[2][2] = r2[0] & 0xffff;
    y[3][0] = r2[0] >> 16; y[3][1] = r2[1] & 0xffff; y[3][2] = r2[1] >> 16;
  } else {
    for (int i = 0; i < 4; ++i) {
      const bool v = i < nmine; const int e = v ? e0 + i : 0;
      d[i] = v ? dst[e] : 0; s[i] = v ? src[e] : 0; a[i] = v ? ea[e] : 0;
      for (int j = 0; j < 3; ++j) y[i][j] = v ? rhat[3 * (size_t)e + j] : 0;
    }
  }
  if (t >= 960 && p < 160) {
    const int id = p * 64 + (t - 960);
    const int f = id >> 8, l = (id >> 2) & 63, dd = id & 3;
    const int m = l & 31, h = l >> 5;
    int sig, ap, tau; const u16* W; int wo;
    if (f < 24) { sig = f >> 3; ap = (f >> 1) & 3; tau = f & 1; W = w1; wo = 128 + 64 * sig; }
    else { const int g = f - 24; sig = g >> 3; ap = (g >> 1) & 3; tau = g & 1; W = w0; wo = 64 * sig; }
    const int c = 32 * h + 8 * ap + 2 * dd, cp = 32 * tau + m;
    const float v0 = bf2f(wrad[wo + c]) * bf2f(W[(64 * sig + c) * 64 + cp]);
    const float v1 = bf2f(wrad[wo + c + 1]) * bf2f(W[(64 * sig + c + 1) * 64 + cp]);
    wfrag[id] = pk2(v0, v1);
  }
  *(u32x4*)&cnt[4 * t] = u32x4{0, 0, 0, 0};
  __syncthreads();
  u32 b[4], r[4];
  for (int i = 0; i < 4; ++i) {
    b[i] = d[i] / NB;
    if (i < nmine) r[i] = atomicAdd(&cnt[wv * NBK + b[i]], 1u);
  }
  __syncthreads();
  u32 tot = 0, incl = 0;
  if (t < NBK) {
    for (int w = 0; w < 16; ++w) { const u32 v = cnt[w * NBK + t]; cnt[w * NBK + t] = tot; tot += v; }
    incl = wave_incl_scan(tot);
    if (lane == 63) wtot[wv] = incl;
  }
  __syncthreads();
  if (t < NBK) {
    u32 base = incl - tot;
    for (int w = 0; w < 4; ++w) base += (w < wv) ? wtot[w] : 0;
    sbase[t] = base;
    offs[p * NBK + t] = base | ((base + tot) << 16);
  }
  __syncthreads();
  for (int i = 0; i < 4; ++i) {
    if (i < nmine) {
      const u32 pos = sbase[b[i]] + cnt[wv * NBK + b[i]] + r[i];
      const float af = bf2f(a[i]);
      skey[pos] = (s[i] << 9) | (d[i] - b[i] * NB);
      spay[pos] = u32x2{a[i] | (pk2(af * bf2f(y[i][0]), 0.f) << 16), pk2(af * bf2f(y[i][1]), af * bf2f(y[i][2]))};
    }
  }
  __syncthreads();
  for (int i = t; i < nval; i += 1024) {
    const u32x2 py = spay[i];
    u32* o = rec + 3 * ((size_t)p * CH + i);
    o[0] = skey[i]; o[1] = py[0]; o[2] = py[1];
  }
}

constexpr int L_WF = 0;
constexpr int L_KEY = 40960;
constexpr int NSLOT = CAP + 256;
constexpr int L_PAY = L_KEY + NSLOT * 4;
constexpr int L_ZERO = L_PAY + NSLOT * 8;
constexpr int L_SEGB = L_ZERO + 16;
constexpr int L_PRE = L_SEGB + 1024;
constexpr int L_SEGID = L_PRE + 1088;
constexpr int L_CNT = L_SEGID + CAP;
constexpr int L_BIN = L_CNT + 4096 + 16;
constexpr int L_WB = L_BIN + 512;
constexpr int L_MISC = L_WB + 64;
constexpr int L_END1 = L_MISC + 64;
constexpr int L_OUT = L_KEY;
constexpr int L_END2 = L_OUT + 8 * 32 * 68 * 4;
constexpr int L_TOTAL = (L_END1 > L_END2 ? L_END1 : L_END2);
constexpr u32 PADKEY = 0x7FFFFE00u | 63u;

__global__ __launch_bounds__(512, 2) void k_agg(const u16* __restrict__ x, const u32* __restrict__ rec, const u32* __restrict__ offs,
                                                const u32* __restrict__ wfrag, const u16* __restrict__ eps, int N, int CH, int P,
                                                u16* __restrict__ out) {
  extern __shared__ __attribute__((aligned(16))) char lds[];
  const int t = threadIdx.x, lane = t & 63, wv = __builtin_amdgcn_readfirstlane(t >> 6);
  const int bkt = (blockIdx.x & 7) * 32 + (blockIdx.x >> 3);
  const int node0 = bkt * NB;
  if (node0 >= N) return;
  const int nn = min(NB, N - node0);
  u32* keys = (u32*)(lds + L_KEY); u32x2* pay = (u32x2*)(lds + L_PAY);
  u32* segb = (u32*)(lds + L_SEGB); u32* pre = (u32*)(lds + L_PRE); unsigned char* segid = (unsigned char*)(lds + L_SEGID);
  u32* cntb = (u32*)(lds + L_CNT); u32* binb = (u32*)(lds + L_BIN); u32* wb = (u32*)(lds + L_WB); u32* misc = (u32*)(lds + L_MISC);

  u32 o = 0;
  if (t < P) o = offs[t * NBK + bkt];
  u32x4 wf[5];
  for (int i = 0; i < 5; ++i) wf[i] = ((const u32x4*)wfrag)[t + 512 * i];
  const u32 sstart = o & 0xffff, slen = (o >> 16) - sstart;
  u32 incl = 0;
  if (t < 256) { incl = wave_incl_scan(slen); if (lane == 63) misc[wv] = incl; }
  if (t == 0) { *(u32x4*)(lds + L_ZERO) = u32x4{0, 0, 0, 0}; }
  for (int i = 0; i < 5; ++i) ((u32x4*)(lds + L_WF))[t + 512 * i] = wf[i];
  __syncthreads();
  u32 spre = 0;
  if (t < 256) {
    u32 base = incl - slen;
    for (int w = 0; w < 4; ++w) base += (w < wv) ? misc[w] : 0;
    spre = base;
    pre[t] = base; segb[t] = (u32)t * CH + sstart - base;
  }
  const u32 total = misc[0] + misc[1] + misc[2] + misc[3];

  f32x16 acc[8];
  for (int j = 0; j < 8; ++j) for (int r = 0; r < 16; ++r) acc[j][r] = 0.f;

  const int h = lane >> 5, m = lane & 31;
  const __amdgpu_buffer_rsrc_t rs = __builtin_amdgcn_make_buffer_rsrc((void*)x, 0, N * 512, 0x00020000);
  const u32 moff = m * 16;
  const u32 ldsbase = (u32)(size_t)lds;
  const int rot = 2 * (blockIdx.x & 7);
  const u32 rmul = (u32)(((unsigned long long)NR << 32) / (u32)N);
  const int sup_r = (lane >> 2) & 3;
  const u32 mynode = 7 * wv + 4 * ((lane >> 4) & 1) + (lane & 3);
  const bool slot_ok = (4 * ((lane >> 4) & 1) + (lane & 3)) < 7;

  const __amdgpu_buffer_rsrc_t rrs = __builtin_amdgcn_make_buffer_rsrc((void*)rec, 0, P * CH * 12, 0x00020000);
  for (u32 r0 = 0; r0 < total; r0 += CAP) {
    const u32 rcnt = min((u32)CAP, total - r0);
    __syncthreads();
    {
      const int sg = t & 255, half = t >> 8;
      const u32 sp = pre[sg];
      const u32 se = (sg == 255) ? total : pre[sg + 1];
      const u32 mid = sp + ((se - sp + 1) >> 1);
      u32 lo = half ? mid : sp, hi = half ? se : mid;
      lo = max(lo, r0); hi = min(hi, r0 + rcnt);
      for (u32 i = lo; i < hi; ++i) segid[i - r0] = (unsigned char)sg;
      cntb[t] = 0; cntb[t + 512] = 0;
    }
    __syncthreads();
    u32 k[8], py0[8], py1[8], br[8];
    #pragma unroll
    for (int i = 0; i < 8; ++i) {
      const u32 r = t + 512 * i;
      const u32 g = segb[segid[r]] + r0 + r;
      const u32 off = (r < rcnt) ? g * 12 : 0x7ffffff0u;
      const __attribute__((ext_vector_type(3))) u32 v = __builtin_amdgcn_raw_buffer_load_b96(rrs, off, 0, 0);
      k[i] = v[0]; py0[i] = v[1]; py1[i] = v[2];
    }
    #pragma unroll
    for (int i = 0; i < 8; ++i) {
      const u32 r = t + 512 * i;
      const u32 nl = k[i] & 63, sr = k[i] >> 9;
      const u32 rng = (__umulhi(sr, rmul) - rot) & (NR - 1);
      const u32 bin = (r < rcnt) ? ((nl * 37) >> 8) * NR + rng : 1024 - wv * 128;
      br[i] = (bin << 16) | atomicAdd(&cntb[wv * 128 + bin], 1u);
    }
    __syncthreads();
    if (wv == 0) {
      u32 t0 = 0, t1 = 0;
      #pragma unroll
      for (int w = 0; w < 8; ++w) {
        u32x2* cp = (u32x2*)&cntb[w * 128 + 2 * lane];
        const u32x2 v = *cp; *cp = u32x2{t0, t1}; t0 += v[0]; t1 += v[1];
      }
      const u32 ps = t0 + t1;
      const u32 S = wave_incl_scan(ps);
      u32 run = 0, prevS = 0, adjv = 0, lbv = 0, lnv = 0;
      #pragma unroll
      for (int g = 0; g < 8; ++g) {
        const u32 endS = __builtin_amdgcn_readlane(S, 8 * g + 7);
        const u32 len = endS - prevS;
        adjv = ((lane >> 3) == g) ? run - prevS : adjv;
        lbv = (lane == g) ? run : lbv; lnv = (lane == g) ? len : lnv;
        run += ((len + 15) & ~15u) + 16; prevS = endS;
      }
      *(u32x2*)&binb[2 * lane] = u32x2{adjv + S - ps, adjv + S - ps + t0};
      if (lane < 8) { wb[lane] = lbv; wb[8 + lane] = lnv; }
    }
    __syncthreads();
    #pragma unroll
    for (int i = 0; i < 8; ++i) {
      const u32 r = t + 512 * i;
      const u32 bin = br[i] >> 16;
      u32 pos = binb[bin & 127] + cntb[(wv * 128 + bin) & 1023] + (br[i] & 0xffff);
      pos = (r < rcnt) ? pos : NSLOT - 1;
      keys[pos] = k[i]; pay[pos] = u32x2{py0[i], py1[i]};
    }
    const u32 mylb = __builtin_amdgcn_readfirstlane(wb[wv]);
    const u32 mycnt = __builtin_amdgcn_readfirstlane(wb[8 + wv]);
    const u32 nsteps = (mycnt + 15) >> 4;
    if (lane < 32) { const u32 i = mycnt + lane; if (i < nsteps * 16 + 16) { keys[mylb + i] = PADKEY; pay[mylb + i] = u32x2{0, 0}; } }
    __syncthreads();

    if (nsteps > 0) {
      const u32* kp = keys + mylb + 8 * h;
      const u32* ks = keys + mylb + 8 * h + sup_r;
      const u32 payaddr = ldsbase + L_PAY + (mylb + 8 * h + sup_r) * 8;
      const u32 zaddr = ldsbase + L_ZERO;
      u32x4 R[8];
      {
        const u32x4 ka = *(const u32x4*)kp, kb = *(const u32x4*)(kp + 4);
        for (int i = 0; i < 4; ++i) R[i] = __builtin_amdgcn_raw_buffer_load_b128(rs, (ka[i] & ~511u) | moff, 0, 0);
        for (int i = 0; i < 4; ++i) R[4 + i] = __builtin_amdgcn_raw_buffer_load_b128(rs, (kb[i] & ~511u) | moff, 0, 0);
      }
      for (u32 s = 0; s < nsteps; ++s) {
        const u32x4 ka = *(const u32x4*)(kp + 16 * (s + 1)), kb = *(const u32x4*)(kp + 16 * (s + 1) + 4);
        const u32 k0 = ks[16 * s], k1 = ks[16 * s + 4];
        const u32 a0 = ((k0 & 63) == mynode && slot_ok) ? payaddr + s * 128 : zaddr;
        const u32 a1 = ((k1 & 63) == mynode && slot_ok) ? payaddr + s * 128 + 32 : zaddr;
        const s16x4 c0 = __builtin_amdgcn_ds_read_tr16_b64_v4i16((LDSP(s16x4))(uintptr_t)a0);
        const s16x4 c1 = __builtin_amdgcn_ds_read_tr16_b64_v4i16((LDSP(s16x4))(uintptr_t)a1);
        const bf16x8 cf = __builtin_bit_cast(bf16x8, __builtin_shufflevector(c0, c1, 0, 1, 2, 3, 4, 5, 6, 7));
        u32x4 F[8];
        #pragma unroll
        for (int dd = 0; dd < 4; ++dd) {
          #pragma unroll
          for (int j = 0; j < 8; ++j)
            F[j][dd] = __builtin_amdgcn_perm(R[2 * dd + 1][j >> 1], R[2 * dd][j >> 1], (j & 1) ? 0x07060302u : 0x05040100u);
          __builtin_amdgcn_sched_barrier(0);
          const u32 ke = dd < 2 ? ka[2 * dd] : kb[2 * dd - 4], ko = dd < 2 ? ka[2 * dd + 1] : kb[2 * dd - 3];
          R[2 * dd] = __builtin_amdgcn_raw_buffer_load_b128(rs, (ke & ~511u) | moff, 0, 0);
          R[2 * dd + 1] = __builtin_amdgcn_raw_buffer_load_b128(rs, (ko & ~511u) | moff, 0, 0);
          __builtin_amdgcn_sched_barrier(0);
        }
        #pragma unroll
        for (int j = 0; j < 8; ++j) acc[j] = __builtin_amdgcn_mfma_f32_32x32x16_bf16(__builtin_bit_cast(bf16x8, F[j]), cf, acc[j], 0, 0, 0);
        __builtin_amdgcn_sched_barrier(0);
      }
    }
  }

  const int q = lane & 3;
  float kap[9];
  for (int j = 0; j < 3; ++j) for (int kk = 0; kk < 3; ++kk)
    { const float ev = bf2f(eps[(q > 0 ? q - 1 : 0) * 9 + 3 * j + kk]); kap[3 * j + kk] = (q == 0) ? (j == kk ? 1.f : 0.f) : ev; }
  const float dl1 = q == 1 ? 1.f : 0.f, dl2 = q == 2 ? 1.f : 0.f, dl3 = q == 3 ? 1.f : 0.f;
  f32x16 D1[2], D2[2];
  for (int i = 0; i < 2; ++i) for (int r = 0; r < 16; ++r) { D1[i][r] = 0.f; D2[i][r] = 0.f; }
  const char* wfl = lds + L_WF + lane * 16;
  #pragma unroll
  for (int a = 0; a < 4; ++a) {
    u32x4 Xa, Xb, Xc;
    #pragma unroll
    for (int jp = 0; jp < 4; ++jp) {
      float va[2], vb[2], vc[2];
      #pragma unroll
      for (int u = 0; u < 2; ++u) {
        const int j = 2 * jp + u;
        const float t0 = acc[j][a], t1 = acc[j][4 + a], t2 = acc[j][8 + a], t3 = acc[j][12 + a];
        va[u] = t0;
        vb[u] = dl1 * dppf<0x00>(t1) + dl2 * dppf<0x00>(t2) + dl3 * dppf<0x00>(t3);
        float c = kap[0] * dppf<0x55>(t1);
        c = fmaf(kap[1], dppf<0x55>(t2), c); c = fmaf(kap[2], dppf<0x55>(t3), c);
        c = fmaf(kap[3], dppf<0xAA>(t1), c); c = fmaf(kap[4], dppf<0xAA>(t2), c); c = fmaf(kap[5], dppf<0xAA>(t3), c);
        c = fmaf(kap[6], dppf<0xFF>(t1), c); c = fmaf(kap[7], dppf<0xFF>(t2), c); c = fmaf(kap[8], dppf<0xFF>(t3), c);
        vc[u] = c;
      }
      Xa[jp] = pk2(va[0], va[1]); Xb[jp] = pk2(vb[0], vb[1]); Xc[jp] = pk2(vc[0], vc[1]);
    }
    __builtin_amdgcn_sched_barrier(0);
    const bf16x8 xa = __builtin_bit_cast(bf16x8, Xa), xb = __builtin_bit_cast(bf16x8, Xb), xc = __builtin_bit_cast(bf16x8, Xc);
    #pragma unroll
    for (int tau = 0; tau < 2; ++tau) {
      D1[tau] = __builtin_amdgcn_mfma_f32_32x32x16_bf16(*(const bf16x8*)(wfl + ((0 * 4 + a) * 2 + tau) * 1024), xa, D1[tau], 0, 0, 0);
      D1[tau] = __builtin_amdgcn_mfma_f32_32x32x16_bf16(*(const bf16x8*)(wfl + ((1 * 4 + a) * 2 + tau) * 1024), xb, D1[tau], 0, 0, 0);
      D1[tau] = __builtin_amdgcn_mfma_f32_32x32x16_bf16(*(const bf16x8*)(wfl + ((2 * 4 + a) * 2 + tau) * 1024), xc, D1[tau], 0, 0, 0);
      D2[tau] = __builtin_amdgcn_mfma_f32_32x32x16_bf16(*(const bf16x8*)(wfl + (24 + (0 * 4 + a) * 2 + tau) * 1024), xa, D2[tau], 0, 0, 0);
      D2[tau] = __builtin_amdgcn_mfma_f32_32x32x16_bf16(*(const bf16x8*)(wfl + (24 + (1 * 4 + a) * 2 + tau) * 1024), xc, D2[tau], 0, 0, 0);
    }
    __builtin_amdgcn_sched_barrier(0);
  }
  __syncthreads();
  {
    float* ob = (float*)(lds + L_OUT) + (wv * 32 + m) * 68 + 4 * h;
    #pragma unroll
    for (int tau = 0; tau < 2; ++tau)
      #pragma unroll
      for (int g4 = 0; g4 < 4; ++g4) {
        f32x4 v;
        for (int u = 0; u < 4; ++u) v[u] = (q == 0) ? D2[tau][4 * g4 + u] : D1[tau][4 * g4 + u];
        *(f32x4*)(ob + 32 * tau + 8 * g4) = v;
      }
  }
  __syncthreads();
  if (t < nn * 8) {
    const int v = t >> 3, k8 = t & 7;
    const int w = v / 7, sl = v - 7 * w;
    const float* ob = (const float*)(lds + L_OUT) + (w * 32 + 4 * sl) * 68 + 8 * k8;
    float gate[8];
    u16* op = out + (size_t)(node0 + v) * 256 + 8 * k8;
    {
      const f32x4 u0 = *(const f32x4*)ob, u1 = *(const f32x4*)(ob + 4);
      for (int i = 0; i < 4; ++i) { gate[i] = 1.f / (1.f + __expf(-u0[i])); gate[4 + i] = 1.f / (1.f + __expf(-u1[i])); }
      *(u32x4*)op = u32x4{pk2(u0[0], u0[1]), pk2(u0[2], u0[3]), pk2(u1[0], u1[1]), pk2(u1[2], u1[3])};
    }
    for (int i = 1; i < 4; ++i) {
      const f32x4 u0 = *(const f32x4*)(ob + 68 * i), u1 = *(const f32x4*)(ob + 68 * i + 4);
      *(u32x4*)(op + 64 * i) = u32x4{pk2(u0[0] * gate[0], u0[1] * gate[1]), pk2(u0[2] * gate[2], u0[3] * gate[3]),
                                     pk2(u1[0] * gate[4], u1[1] * gate[5]), pk2(u1[2] * gate[6], u1[3] * gate[7])};
    }
  }
}

extern "C" void kernel_launch(void* const* d_in, const int* in_sizes, int n_in, void* d_out, int out_size, void* d_ws, size_t ws_size,
                              hipStream_t stream) {
  const u16* x = (const u16*)d_in[0]; const u16* rhat = (const u16*)d_in[1]; const u16* ea = (const u16*)d_in[2];
  const int* src = (const int*)d_in[3]; const int* dst = (const int*)d_in[4];
  const u16* wrad = (const u16*)d_in[5]; const u16* w0 = (const u16*)d_in[6]; const u16* w1 = (const u16*)d_in[7]; const u16* eps = (const u16*)d_in[8];
  const int N = in_sizes[0] / 256, E = in_sizes[3];
  const int CH = ((E + PA - 1) / PA + 3) & ~3;
  u32* offs = (u32*)d_ws;
  u32* wfrag = offs + PA * NBK;
  u32* rec = wfrag + 10240;
  k_part<<<PA, 1024, 0, stream>>>(src, dst, rhat, ea, wrad, w0, w1, E, CH, rec, offs, wfrag);
  hipFuncSetAttribute((const void*)k_agg, hipFuncAttributeMaxDynamicSharedMemorySize, L_TOTAL);
  k_agg<<<256, 512, L_TOTAL, stream>>>(x, rec, offs, wfrag, eps, N, CH, PA, (u16*)d_out);
}
```

```cpp
#include <hip/hip_runtime.h>
#include <stdint.h>

typedef unsigned short u16;
typedef unsigned u32;
typedef unsigned long long u64;
typedef __attribute__((ext_vector_type(4))) unsigned u32x4;
typedef __attribute__((ext_vector_type(2))) unsigned u32x2;
typedef __attribute__((ext_vector_type(4))) float f32x4;
typedef __attribute__((ext_vector_type(16))) float f32x16;
typedef __attribute__((ext_vector_type(8))) __bf16 bf16x8;
typedef __attribute__((ext_vector_type(4))) short s16x4;
typedef __attribute__((ext_vector_type(2))) __bf16 bf16x2;
#define LDSP(T) __attribute__((address_space(3))) T*
#define GASP(T) __attribute__((address_space(1))) T*

#define STAMP(i)
#define STAMP_FLUSH()
#define PRIO 1
#define NPF 0

constexpr int NB = 49;
constexpr int NWG = 256;
constexpr int CHMAX = 3136;
constexpr int CAP = 4096;
constexpr int NR = 16;
constexpr int NSLOT = CAP + 256;

constexpr int L_W = 0;
constexpr int L_R = 40960;
constexpr int LA_CNT = L_R;
constexpr int LA_KEY = LA_CNT + 8192;
constexpr int LA_PAY = LA_KEY + CHMAX * 4;
constexpr int LA_TOT = LA_PAY + CHMAX * 8;
constexpr int LA_END = LA_TOT + 16;
constexpr int LB_KEY = L_R;
constexpr int LB_PAY = LB_KEY + NSLOT * 4;
constexpr int LB_MARK = LB_PAY + NSLOT * 8;
constexpr int LB_NCNT = LB_MARK + 8 * 64;
constexpr int LB_CNT = LB_NCNT + 2048 + 16;
constexpr int LB_TAB = LB_CNT + 4096 + 16;
constexpr int LB_END = LB_TAB + 4096;
constexpr int LE_OUT = L_R;
constexpr int LE_END = LE_OUT + 8 * 32 * 68 * 4;
constexpr int cmax(int a, int b) { return a > b ? a : b; }
constexpr int L_ZERO = cmax(cmax(LA_END, LB_END), LE_END);
constexpr int L_MISC = L_ZERO + 16;
constexpr int L_KAP = L_MISC + 64;
constexpr int L_NINF = L_KAP + 192;
constexpr int L_FN = L_NINF + 256;
constexpr int L_TOTAL = L_FN + 32;
constexpr u32 PADKEY = 0x7FFFFE00u | 63u;
constexpr u32 OOB = 0x7ffffff0u;

constexpr size_t W_EPOCH = 0;
constexpr size_t W_GRAN = 256;
constexpr size_t W_KEY = W_GRAN + (size_t)NWG * NWG * 8;
constexpr size_t W_PAY = W_KEY + (size_t)NWG * CHMAX * 4;

__device__ __forceinline__ float bf2f(u32 v) { return __uint_as_float(v << 16); }
__device__ __forceinline__ float bfhi(u32 v) { return __uint_as_float(v & 0xffff0000u); }
__device__ __forceinline__ u32 pk2(float lo, float hi) {
  bf16x2 v; v[0] = (__bf16)lo; v[1] = (__bf16)hi; return __builtin_bit_cast(u32, v);
}
__device__ __forceinline__ void opaque(u32& v) { asm volatile("" : "+v"(v)); }
__device__ __forceinline__ u32 wave_incl_scan(u32 v) {
  v += (u32)__builtin_amdgcn_update_dpp(0, (int)v, 0x111, 0xf, 0xf, true);
  v += (u32)__builtin_amdgcn_update_dpp(0, (int)v, 0x112, 0xf, 0xf, true);
  v += (u32)__builtin_amdgcn_update_dpp(0, (int)v, 0x114, 0xf, 0xf, true);
  v += (u32)__builtin_amdgcn_update_dpp(0, (int)v, 0x118, 0xf, 0xf, true);
  v += (u32)__builtin_amdgcn_update_dpp(0, (int)v, 0x142, 0xa, 0xf, false);
  v += (u32)__builtin_amdgcn_update_dpp(0, (int)v, 0x143, 0xc, 0xf, false);
  return v;
}
#define DPPOP(name, qp) \
  __device__ __forceinline__ float name##_mul(float s, float k) { float d; asm("v_mul_f32_dpp %0, %1, %2 quad_perm:" qp " row_mask:0xf bank_mask:0xf" : "=v"(d) : "v"(s), "v"(k)); return d; } \
  __device__ __forceinline__ void name##_fma(float& d, float s, float k) { asm("v_fmac_f32_dpp %0, %1, %2 quad_perm:" qp " row_mask:0xf bank_mask:0xf" : "+v"(d) : "v"(s), "v"(k)); }
DPPOP(q0, "[0,0,0,0]") DPPOP(q1, "[1,1,1,1]") DPPOP(q2, "[2,2,2,2]") DPPOP(q3, "[3,3,3,3]")

#define RSRC(p, bytes) __builtin_amdgcn_make_buffer_rsrc((void*)(p), 0, (bytes), 0x00020000)
constexpr int SC1 = 16;

template <int MODE>
__global__ __launch_bounds__(512, 2) void k_main(const u16* __restrict__ x, const u16* __restrict__ rhat, const u16* __restrict__ ea,
                                                 const int* __restrict__ src, const int* __restrict__ dst, const u16* __restrict__ wrad,
                                                 const u16* __restrict__ w0, const u16* __restrict__ w1, const u16* __restrict__ eps,
                                                 u16* __restrict__ out, char* __restrict__ ws, int N, int E, int CH, u32 rmul
                                                 ) {
  extern __shared__ __attribute__((aligned(16))) char lds[];
  const int t = threadIdx.x, lane = t & 63, wv = __builtin_amdgcn_readfirstlane(t >> 6);
  const int xcd = blockIdx.x & 7, rank = blockIdx.x >> 3;
  const u32 ldsbase = (u32)(size_t)lds;
  constexpr int AUX = MODE == 0 ? SC1 : 0;
  STAMP(0);
  const __amdgpu_buffer_rsrc_t rkey = RSRC(ws + W_KEY, NWG * CH * 4), rpay = RSRC(ws + W_PAY, NWG * CH * 8);

  const int p = blockIdx.x;
  const int nval = min(CH, E - p * CH);
  const int nmine = min(8, nval - 8 * t);
  u32x4 dv[2], sv[2], av, rv[3];
  if (MODE != 2) {
    const u32 e0 = p * CH + 8 * t;
    const bool any = nmine > 0;
    const __amdgpu_buffer_rsrc_t rd = RSRC(dst, E * 4), rsr = RSRC(src, E * 4), ra = RSRC(ea, (E * 2 + 3) & ~3), rr = RSRC(rhat, (E * 6 + 3) & ~3);
    const u32 o4 = any ? e0 * 4 : OOB, o2 = any ? e0 * 2 : OOB, o6 = any ? e0 * 6 : OOB;
    dv[0] = __builtin_amdgcn_raw_buffer_load_b128(rd, o4, 0, 0); dv[1] = __builtin_amdgcn_raw_buffer_load_b128(rd, o4 + 16, 0, 0);
    sv[0] = __builtin_amdgcn_raw_buffer_load_b128(rsr, o4, 0, 0); sv[1] = __builtin_amdgcn_raw_buffer_load_b128(rsr, o4 + 16, 0, 0);
    av = __builtin_amdgcn_raw_buffer_load_b128(ra, o2, 0, 0);
    rv[0] = __builtin_amdgcn_raw_buffer_load_b128(rr, o6, 0, 0); rv[1] = __builtin_amdgcn_raw_buffer_load_b128(rr, o6 + 16, 0, 0);
    rv[2] = __builtin_amdgcn_raw_buffer_load_b128(rr, o6 + 32, 0, 0);
    __builtin_amdgcn_sched_barrier(0);
  }
  u64 ep = 0;
  if (MODE == 0 && t == 511) ep = __hip_atomic_load((GASP(u64))(ws + W_EPOCH), __ATOMIC_RELAXED, __HIP_MEMORY_SCOPE_AGENT);
  u32x4 wr[10]; u32 wsc[10]; u32 pf[4] = {0, 0, 0, 0}; u32 epsv = 0;
  const int u4 = t - 256;
  auto side_loads = [&]() {
    epsv = eps[(u4 >= 9 && u4 < 36) ? u4 - 9 : 0];
    #pragma unroll
    for (int i = 0; i < 10; ++i) {
      const int r = (u4 >> 3) + 32 * i;
      const u16* wp = i < 6 ? w1 + r * 64 : w0 + (r - 192) * 64;
      wr[i] = *(const u32x4*)(wp + 8 * (u4 & 7));
      wsc[i] = wrad[i < 6 ? r + 128 : r - 192];
    }
    __builtin_amdgcn_sched_barrier(0);
    const u32 xb = (u32)N * 512, st0 = ((u32)xcd * N >> 3) * 512;
    #pragma unroll
    for (int i = 0; i < 4; ++i) {
      const u32 li = u4 + 256 * i;
      u32 o = st0 + (rank * NPF + li) * 128; o = o >= xb ? o - xb : o;
      if (256 * i < NPF) pf[i] = __builtin_amdgcn_raw_buffer_load_b32(RSRC(x, xb), li < NPF ? o : OOB, 0, 0);
    }
    __builtin_amdgcn_sched_barrier(0);
  };
  auto weights_to_lds = [&]() {
    #pragma unroll
    for (int i = 0; i < 10; ++i) {
      const int r = (u4 >> 3) + 32 * i;
      opaque(wsc[i]);
      const float s = bf2f(wsc[i]);
      u32x4 v;
      #pragma unroll
      for (int j = 0; j < 4; ++j) v[j] = pk2(s * bf2f(wr[i][j]), s * bfhi(wr[i][j]));
      *(u32x4*)(lds + L_W + r * 128 + (((u4 & 7) ^ (((r >> 1) & 1) << 2)) << 4)) = v;
    }
    opaque(epsv);
    if (u4 < 36) {
      const int qq = u4 / 9, jk = u4 - 9 * qq;
      ((float*)(lds + L_KAP))[qq * 12 + jk] = qq == 0 ? ((jk == 0 || jk == 4 || jk == 8) ? 1.f : 0.f) : bf2f(epsv);
    }
    asm volatile("" :: "v"(pf[0]), "v"(pf[1]), "v"(pf[2]), "v"(pf[3]));
    if (u4 == 0) *(u32x4*)(lds + L_ZERO) = u32x4{0, 0, 0, 0};
  };
  if (MODE == 2 && wv >= 4) side_loads();

  u32 gval = 0; u64 tag = 0;
  if (MODE != 2) {
    u32* cnt = (u32*)(lds + LA_CNT); u32* skey = (u32*)(lds + LA_KEY); u32x2* spay = (u32x2*)(lds + LA_PAY); u32* wtot = (u32*)(lds + LA_TOT);
    *(u32x4*)&cnt[4 * t] = u32x4{0, 0, 0, 0};
    __syncthreads();
    STAMP(1);
    u32 ci[8], rk[8], nl[8];
    #pragma unroll
    for (int i = 0; i < 8; ++i) {
      const u32 d = dv[i >> 2][i & 3];
      const u32 b = d / NB;
      nl[i] = d - b * NB;
      ci[i] = b * 8 + (wv ^ ((b >> 2) & 7));
      rk[i] = 0;
      if (i < nmine) rk[i] = atomicAdd(&cnt[ci[i]], 1u);
    }
    u32 c01[8], c23[8];
    #pragma unroll
    for (int i = 0; i < 8; ++i) {
      const u32 aw = av[i >> 1]; const float a = (i & 1) ? bfhi(aw) : bf2f(aw);
      float y[3];
      #pragma unroll
      for (int j = 0; j < 3; ++j) { const int id = 3 * i + j; const u32 w = rv[id >> 3][(id >> 1) & 3]; y[j] = (id & 1) ? bfhi(w) : bf2f(w); }
      c01[i] = pk2(a, a * y[0]); c23[i] = pk2(a * y[1], a * y[2]);
      opaque(c01[i]); opaque(c23[i]);
    }
    u32 kk[8];
    #pragma unroll
    for (int i = 0; i < 8; ++i) { kk[i] = (sv[i >> 2][i & 3] << 9) | nl[i]; opaque(kk[i]); }
    if (MODE == 0 && t == 511) *(u64*)(lds + L_MISC + 32) = (ep + 1) & 0xffffffffffull;
    __syncthreads();
    STAMP(2);
    u32x4 ca, cb; u32 tot = 0, incl = 0;
    if (MODE == 0 && wv >= 4) side_loads();
    if (t < NWG) {
      ca = *(const u32x4*)&cnt[8 * t]; cb = *(const u32x4*)&cnt[8 * t + 4];
      u32 run = 0, v;
      #pragma unroll
      for (int i = 0; i < 4; ++i) { v = ca[i]; ca[i] = run; run += v; }
      #pragma unroll
      for (int i = 0; i < 4; ++i) { v = cb[i]; cb[i] = run; run += v; }
      tot = run; incl = wave_incl_scan(tot);
      if (lane == 63) wtot[wv] = incl;
    }
    __syncthreads();
    if (t < NWG) {
      const u32x4 wt = *(const u32x4*)wtot;
      const u32 base = incl - tot + (wv > 0 ? wt[0] : 0) + (wv > 1 ? wt[1] : 0) + (wv > 2 ? wt[2] : 0);
      *(u32x4*)&cnt[8 * t] = ca + base; *(u32x4*)&cnt[8 * t + 4] = cb + base;
      gval = base | ((base + tot) << 12);
    }
    __syncthreads();
    STAMP(3);
    #pragma unroll
    for (int i = 0; i < 8; ++i) {
      u32 pos = cnt[ci[i]] + rk[i]; opaque(pos);
      pos = (i < nmine) ? pos : CHMAX - 1;
      skey[pos] = kk[i];
      spay[pos] = u32x2{c01[i], c23[i]};
    }
    __syncthreads();
    STAMP(4);
    {
      const u32 nk = (nval > 0 ? nval : 0) * 4, np = nk * 2;
      #pragma unroll
      for (int i = 0; i < 2; ++i) {
        const u32 o = (t + 512 * i) * 16;
        const u32x4 v = *(const u32x4*)(lds + LA_KEY + (o < CHMAX * 4 ? o : 0));
        __builtin_amdgcn_raw_buffer_store_b128(v, rkey, o < nk ? p * CH * 4 + o : OOB, 0, AUX);
      }
      #pragma unroll
      for (int i = 0; i < 4; ++i) {
        const u32 o = (t + 512 * i) * 16;
        const u32x4 v = *(const u32x4*)(lds + LA_PAY + (o < CHMAX * 8 ? o : 0));
        __builtin_amdgcn_raw_buffer_store_b128(v, rpay, o < np ? p * CH * 8 + o : OOB, 0, AUX);
      }
    }
    STAMP(5);
    if (MODE == 0) {
      __builtin_amdgcn_sched_barrier(0);
      if (wv >= 4) weights_to_lds();
      asm volatile("s_waitcnt vmcnt(0)" ::: "memory");
      __syncthreads();
    }
    if (MODE == 0) tag = *(const u64*)(lds + L_MISC + 32);
    if (t < NWG) {
      GASP(u64) g = (GASP(u64))(ws + W_GRAN) + (size_t)t * NWG + p;
      if (MODE == 0) __hip_atomic_store(g, (tag << 24) | gval, __ATOMIC_RELAXED, __HIP_MEMORY_SCOPE_AGENT);
      else *g = gval;
    }
    STAMP(6);
  }
  if (MODE == 1) { STAMP_FLUSH(); return; }

  if (MODE == 2 && wv >= 4) weights_to_lds();

  const int bkt = xcd * 32 + rank;
  const int node0 = bkt * NB;
  const int nn = min(NB, N - node0);
  u32* keys = (u32*)(lds + LB_KEY); u32x2* pay = (u32x2*)(lds + LB_PAY);
  u32* mark = (u32*)(lds + LB_MARK) + wv * 16;
  u32* ncnt = (u32*)(lds + LB_NCNT); u32* cntb = (u32*)(lds + LB_CNT); u32* tab = (u32*)(lds + LB_TAB) + wv * 128;
  u32* misc = (u32*)(lds + L_MISC);

  f32x16 acc[8];
  #pragma unroll
  for (int j = 0; j < 8; ++j)
    #pragma unroll
    for (int r = 0; r < 16; ++r) acc[j][r] = 0.f;
  const int h = lane >> 5, m = lane & 31;
  const __amdgpu_buffer_rsrc_t rs = RSRC(x, N * 512);
  const u32 moff = m * 16;
  const u32 rot = 2 * xcd;
  const int sup_r = (lane >> 2) & 3;
  const u32 myslot = 4 * ((lane >> 4) & 1) + (lane & 3);
  u32* ninf = (u32*)(lds + L_NINF); u32* fnl = (u32*)(lds + L_FN);
  auto clear_round = [&]() {
    if (lane < 16) mark[lane] = 0;
    cntb[t] = 0; cntb[t + 512] = 0; ncnt[t] = 0;
  };
  clear_round();
  asm volatile("" : "+v"(acc[0]), "+v"(acc[1]), "+v"(acc[2]), "+v"(acc[3]), "+v"(acc[4]), "+v"(acc[5]), "+v"(acc[6]), "+v"(acc[7]));
  __builtin_amdgcn_sched_barrier(0);

  const int sp = 32 * wv + (lane & 31);
  u32 o;
  {
    GASP(u64) g = (GASP(u64))(ws + W_GRAN) + (size_t)bkt * NWG + sp;
    if (MODE == 0) {
      u64 v; u32 spins = 0;
      for (;;) {
        v = __hip_atomic_load(g, __ATOMIC_RELAXED, __HIP_MEMORY_SCOPE_AGENT);
        if (__all((v >> 24) == tag)) break;
        if (++spins > (1u << 17)) break;
        __builtin_amdgcn_s_sleep(1);
      }
      o = (u32)v & 0xffffffu;
    } else o = (u32)*g & 0xffffffu;
  }
  STAMP(7);
  const u32 sstart = o & 0xfff;
  const u32 slen = (lane < 32) ? (o >> 12) - sstart : 0u;
  const u32 sincl = wave_incl_scan(slen);
  const u32 spre = sincl - slen;
  const u32 cntw = __builtin_amdgcn_readlane(sincl, 63);
  const u64 nem = __ballot(slen != 0);
  const u32 nrk = __builtin_amdgcn_mbcnt_hi((u32)(nem >> 32), __builtin_amdgcn_mbcnt_lo((u32)nem, 0));
  const u32 sG = (u32)__builtin_amdgcn_ds_permute((int)((slen != 0 ? nrk : 63u) << 2), (int)((u32)sp * CH + sstart - spre));
  if (lane == 0) misc[wv] = cntw;

  u32 r0 = 0, maxw;
  do {
    { const u32 pb = spre - 1 - r0;
      if (slen != 0 && spre != 0 && pb < 512) atomicOr(&mark[pb >> 5], 1u << (pb & 31)); }
    u32 carry = __builtin_popcountll(__ballot(slen != 0 && spre != 0 && spre <= r0));
    u32x4 mk[4];
    #pragma unroll
    for (int i = 0; i < 4; ++i) mk[i] = *(const u32x4*)&mark[4 * i];
    u32 gi[8];
    #pragma unroll
    for (int i = 0; i < 8; ++i) {
      const u32 lo = mk[i >> 1][2 * (i & 1)], hi = mk[i >> 1][2 * (i & 1) + 1];
      const u32 rkk = __builtin_amdgcn_mbcnt_hi(hi, __builtin_amdgcn_mbcnt_lo(lo, carry));
      carry += __builtin_popcount(lo) + __builtin_popcount(hi);
      gi[i] = (u32)__builtin_amdgcn_ds_bpermute((int)(rkk << 2), (int)sG);
    }
    __builtin_amdgcn_sched_barrier(0);
    u32 k[8], br[8]; u32x2 py[8];
    #pragma unroll
    for (int i = 0; i < 8; ++i) {
      const u32 r = r0 + lane + 64 * i;
      u32 g = gi[i] + r; opaque(g);
      const bool ok = r < cntw;
      k[i] = __builtin_amdgcn_raw_buffer_load_b32(rkey, ok ? g * 4 : OOB, 0, AUX);
      py[i] = __builtin_amdgcn_raw_buffer_load_b64(rpay, ok ? g * 8 : OOB, 0, AUX);
    }
    STAMP(8);
    __syncthreads();
    { const u32x4 m0 = *(const u32x4*)misc, m1 = *(const u32x4*)(misc + 4);
      maxw = __builtin_amdgcn_readfirstlane(max(max(max(m0[0], m0[1]), max(m0[2], m0[3])), max(max(m1[0], m1[1]), max(m1[2], m1[3])))); }
    if (MODE == 0 && blockIdx.x == 0 && t == 0 && r0 == 0) {
      __hip_atomic_store((GASP(u64))(ws + W_EPOCH), tag, __ATOMIC_RELAXED, __HIP_MEMORY_SCOPE_AGENT);
    }
    bool split = maxw <= 512;
    u32 cw[8]; u32 fnv = 0, ninfo = 0;
    if (split) {
      u32 rk1[8];
      #pragma unroll
      for (int i = 0; i < 8; ++i) {
        u32 ix = wv * 64 + (k[i] & 63); opaque(ix);
        ix = (r0 + lane + 64 * i < cntw) ? ix : 512;
        rk1[i] = atomicAdd(&ncnt[ix], 1u);
      }
      __syncthreads();
      STAMP(9);
      u32 c[8];
      #pragma unroll
      for (int w = 0; w < 8; ++w) c[w] = ncnt[w * 64 + lane];
      u32 tot = 0, pre = 0;
      #pragma unroll
      for (int w = 0; w < 8; ++w) { pre = (w == wv) ? tot : pre; tot += c[w]; }
      const u32 S = wave_incl_scan(tot);
      const u32 ns = S - tot, nb = ns + pre;
      const u32 total = __builtin_amdgcn_readlane(S, 63);
      const u32 L = total ? ((total + 127) >> 7) << 4 : 16;
      const float invL = __builtin_amdgcn_rcpf((float)L);
      const u32 wf = (u32)(((float)ns + 0.5f) * invL);
      const u32 wl = (u32)(((float)S - 0.5f) * invL);
      const u32 gl = (wf * L == ns) ? wf : wf + 1;
      const bool has = tot != 0 && gl * L < S;
      fnv = (u32)__builtin_amdgcn_ds_permute((int)((has ? gl : 63u) << 2), (int)lane);
      fnv = (lane * L < total) ? fnv : 64u;
      const u32 f0 = (u32)__builtin_amdgcn_ds_bpermute((int)(wf << 2), (int)fnv);
      split = !__any(tot != 0 && (wl > gl || lane - f0 > 7));
      ninfo = tot ? (wf | ((wl - wf + 1) << 4)) : 0u;
      if (split) {
        #pragma unroll
        for (int i = 0; i < 8; ++i) rk1[i] += (u32)__builtin_amdgcn_ds_bpermute((int)((k[i] & 63) << 2), (int)nb);
        #pragma unroll
        for (int i = 0; i < 8; ++i) cw[i] = (u32)(((float)rk1[i] + 0.5f) * invL);
      }
    }
    if (!split) {
      fnv = 7 * lane; ninfo = ((lane * 37) >> 8) | 16u;
      #pragma unroll
      for (int i = 0; i < 8; ++i) cw[i] = ((k[i] & 63) * 37) >> 8;
    }
    if (wv == 0) { ninf[lane] = ninfo; if (lane < 8) fnl[lane] = fnv; }
    const u32 myfn = __builtin_amdgcn_readlane(fnv, wv);
    #pragma unroll
    for (int i = 0; i < 8; ++i) {
      u32 bin = cw[i] * NR + ((__umulhi(k[i] >> 9, rmul) - rot) & (NR - 1));
      opaque(bin);
      bin = (r0 + lane + 64 * i < cntw) ? bin : 1024 - wv * 128;
      br[i] = (bin << 16) | atomicAdd(&cntb[wv * 128 + bin], 1u);
    }
    __syncthreads();
    STAMP(10);
    u32 mylb, mycnt;
    {
      u32x2 c[8];
      #pragma unroll
      for (int w = 0; w < 8; ++w) c[w] = *(const u32x2*)&cntb[w * 128 + 2 * lane];
      u32 t0 = 0, t1 = 0, p0 = 0, p1 = 0;
      #pragma unroll
      for (int w = 0; w < 8; ++w) { p0 = (w == wv) ? t0 : p0; p1 = (w == wv) ? t1 : p1; t0 += c[w][0]; t1 += c[w][1]; }
      const u32 ps = t0 + t1;
      const u32 S = wave_incl_scan(ps);
      const u32 send = (u32)__builtin_amdgcn_ds_bpermute((lane | 7) << 2, (int)S);
      const u32 gstart = (u32)__builtin_amdgcn_ds_bpermute((lane & ~7) << 2, (int)(S - ps));
      const u32 len = send - gstart;
      const u32 padl = ((lane & 7) == 7) ? ((len + 15) & ~15u) + 16 - len : 0u;
      const u32 P = wave_incl_scan(padl) - padl;
      const u32 padprev = (u32)__builtin_amdgcn_ds_bpermute((lane & ~7) << 2, (int)P);
      const u32 b0 = S - ps + padprev;
      *(u32x2*)&tab[2 * lane] = u32x2{b0 + p0, b0 + t0 + p1};
      mylb = __builtin_amdgcn_readlane(gstart + padprev, 8 * wv);
      mycnt = __builtin_amdgcn_readlane(len, 8 * wv);
    }
    #pragma unroll
    for (int i = 0; i < 8; ++i) br[i] = tab[(br[i] >> 16) & 127] + (br[i] & 0xffff);
    #pragma unroll
    for (int i = 0; i < 8; ++i) {
      u32 pos = br[i]; opaque(pos);
      pos = (r0 + lane + 64 * i < cntw) ? pos : NSLOT - 1;
      keys[pos] = k[i]; pay[pos] = py[i];
    }
    const u32 nsteps = (mycnt + 15) >> 4;
    if (lane < 32) { const u32 i = mycnt + lane; if (i < nsteps * 16 + 16) { keys[mylb + i] = PADKEY; pay[mylb + i] = u32x2{0, 0}; } }
    __syncthreads();
    STAMP(11);

    if (nsteps > 0) {
      const u32 mynode = myfn + myslot;
      const u32* kp = keys + mylb + 8 * h;
      const u32* ks = keys + mylb + 8 * h + sup_r;
      const u32 payaddr = ldsbase + LB_PAY + (mylb + 8 * h + sup_r) * 8;
      const u32 zaddr = ldsbase + L_ZERO;
      u32x4 R[8];
      {
        const u32x4 ka = *(const u32x4*)kp, kb = *(const u32x4*)(kp + 4);
        #pragma unroll
        for (int i = 0; i < 4; ++i) R[i] = __builtin_amdgcn_raw_buffer_load_b128(rs, (ka[i] & ~511u) | moff, 0, 0);
        #pragma unroll
        for (int i = 0; i < 4; ++i) R[4 + i] = __builtin_amdgcn_raw_buffer_load_b128(rs, (kb[i] & ~511u) | moff, 0, 0);
      }
      for (u32 s = 0; s < nsteps; ++s) {
        if (PRIO == 3) __builtin_amdgcn_s_setprio(3);
        if (PRIO == 1) { if (((s + (wv >> 2)) & 1) != 0) __builtin_amdgcn_s_setprio(1); else __builtin_amdgcn_s_setprio(0); }
        if (PRIO == 2) { if (s == 0 && wv >= 4) __builtin_amdgcn_s_setprio(1); }
        const u32x4 ka = *(const u32x4*)(kp + 16 * (s + 1)), kb = *(const u32x4*)(kp + 16 * (s + 1) + 4);
        const u32 k0 = ks[16 * s], k1 = ks[16 * s + 4];
        const u32 a0 = ((k0 & 63) == mynode) ? payaddr + s * 128 : zaddr;
        const u32 a1 = ((k1 & 63) == mynode) ? payaddr + s * 128 + 32 : zaddr;
        const s16x4 c0 = __builtin_amdgcn_ds_read_tr16_b64_v4i16((LDSP(s16x4))(uintptr_t)a0);
        const s16x4 c1 = __builtin_amdgcn_ds_read_tr16_b64_v4i16((LDSP(s16x4))(uintptr_t)a1);
        const bf16x8 cf = __builtin_bit_cast(bf16x8, __builtin_shufflevector(c0, c1, 0, 1, 2, 3, 4, 5, 6, 7));
        u32x4 F[8];
        #pragma unroll
        for (int dd = 0; dd < 4; ++dd) {
          #pragma unroll
          for (int j = 0; j < 8; ++j)
            F[j][dd] = __builtin_amdgcn_perm(R[2 * dd + 1][j >> 1], R[2 * dd][j >> 1], (j & 1) ? 0x07060302u : 0x05040100u);
          __builtin_amdgcn_sched_barrier(0);
          const u32 ke = dd < 2 ? ka[2 * dd] : kb[2 * dd - 4], ko = dd < 2 ? ka[2 * dd + 1] : kb[2 * dd - 3];
          R[2 * dd] = __builtin_amdgcn_raw_buffer_load_b128(rs, (ke & ~511u) | moff, 0, 0);
          R[2 * dd + 1] = __builtin_amdgcn_raw_buffer_load_b128(rs, (ko & ~511u) | moff, 0, 0);
          __builtin_amdgcn_sched_barrier(0);
        }
        if (PRIO == 3) __builtin_amdgcn_s_setprio(0);
        #pragma unroll
        for (int j = 0; j < 8; ++j) acc[j] = __builtin_amdgcn_mfma_f32_32x32x16_bf16(__builtin_bit_cast(bf16x8, F[j]), cf, acc[j], 0, 0, 0);
        __builtin_amdgcn_sched_barrier(0);
      }
    }
    r0 += 512;
    if (r0 < maxw) { __syncthreads(); clear_round(); }
  } while (r0 < maxw);
  if (PRIO) __builtin_amdgcn_s_setprio(0);
  STAMP(12);

  u32 te = t; opaque(te);
  const int q = te & 3;
  float kap[9];
  { const float* kl = (const float*)(lds + L_KAP) + q * 12;
    const f32x4 k0 = *(const f32x4*)kl, k1 = *(const f32x4*)(kl + 4); kap[8] = kl[8];
    #pragma unroll
    for (int i = 0; i < 4; ++i) { kap[i] = k0[i]; kap[4 + i] = k1[i]; } }
  const float dl1 = q == 1 ? 1.f : 0.f, dl2 = q == 2 ? 1.f : 0.f, dl3 = q == 3 ? 1.f : 0.f;
  asm volatile("s_nop 15\n\ts_nop 15" : "+v"(acc[0]), "+v"(acc[1]), "+v"(acc[2]), "+v"(acc[3]), "+v"(acc[4]), "+v"(acc[5]), "+v"(acc[6]), "+v"(acc[7]));
  f32x16 D1[2], D2[2];
  #pragma unroll
  for (int i = 0; i < 2; ++i)
    #pragma unroll
    for (int r = 0; r < 16; ++r) { D1[i][r] = 0.f; D2[i][r] = 0.f; }
  const u32 he = (te >> 5) & 1, me = te & 31;
  const u32 wrow = 32 * he + ((te >> 2) & 3);
  const u32 wa0 = ldsbase + L_W + wrow * 128 + 32 * ((te >> 4) & 1) + 8 * (te & 3) + ((wrow >> 1) & 1) * 64;
  const u32 wa1 = wa0 ^ 64;
  auto wfrag = [&](int g) {
    const int a = g / 10, tau = (g % 10) / 5, kind = g % 5;
    const u32 aa = (tau ? wa1 : wa0) + (64 * kind + 8 * a) * 128;
    const s16x4 lo = __builtin_amdgcn_ds_read_tr16_b64_v4i16((LDSP(s16x4))(uintptr_t)aa);
    const s16x4 hi = __builtin_amdgcn_ds_read_tr16_b64_v4i16((LDSP(s16x4))(uintptr_t)(aa + 512));
    return __builtin_bit_cast(bf16x8, __builtin_shufflevector(lo, hi, 0, 1, 2, 3, 4, 5, 6, 7));
  };
  float vb[2], vc[2];
  auto mix1 = [&](int a, int j, u32x4& Xa, u32x4& Xb, u32x4& Xc) {
    const float t1 = acc[j][4 + a], t2 = acc[j][8 + a], t3 = acc[j][12 + a];
    float bb, c;
    #define QP(n) " quad_perm:[" #n "," #n "," #n "," #n "] row_mask:0xf bank_mask:0xf\n\t"
    asm("v_mul_f32_dpp %0, %2, %5" QP(0) "v_mul_f32_dpp %1, %2, %8" QP(1)
        "v_fmac_f32_dpp %0, %3, %6" QP(0) "v_fmac_f32_dpp %1, %3, %9" QP(1)
        "v_fmac_f32_dpp %0, %4, %7" QP(0) "v_fmac_f32_dpp %1, %4, %10" QP(1)
        "v_fmac_f32_dpp %1, %2, %11" QP(2) "v_fmac_f32_dpp %1, %3, %12" QP(2) "v_fmac_f32_dpp %1, %4, %13" QP(2)
        "v_fmac_f32_dpp %1, %2, %14" QP(3) "v_fmac_f32_dpp %1, %3, %15" QP(3) "v_fmac_f32_dpp %1, %4, %16" QP(3)
        : "=&v"(bb), "=&v"(c)
        : "v"(t1), "v"(t2), "v"(t3), "v"(dl1), "v"(dl2), "v"(dl3), "v"(kap[0]), "v"(kap[1]), "v"(kap[2]), "v"(kap[3]), "v"(kap[4]), "v"(kap[5]),
          "v"(kap[6]), "v"(kap[7]), "v"(kap[8]));
    #undef QP
    vb[j & 1] = bb; vc[j & 1] = c;
    if (j & 1) { Xa[j >> 1] = pk2(acc[j - 1][a], acc[j][a]); Xb[j >> 1] = pk2(vb[0], vb[1]); Xc[j >> 1] = pk2(vc[0], vc[1]); }
  };
  u32x4 X[2][3];
  bf16x8 wf[3];
  wf[0] = wfrag(0); wf[1] = wfrag(1);
  #pragma unroll
  for (int j = 0; j < 8; ++j) mix1(0, j, X[0][0], X[0][1], X[0][2]);
  #pragma unroll
  for (int g = 0; g < 40; ++g) {
    const int a = g / 10, i = g % 10, tau = i / 5, kind = i % 5;
    __builtin_amdgcn_sched_barrier(0);
    if (g + 2 < 40) wf[(g + 2) % 3] = wfrag(g + 2);
    const bf16x8 xb = __builtin_bit_cast(bf16x8, X[a & 1][kind == 1 ? 1 : (kind == 2 || kind == 4) ? 2 : 0]);
    if (kind < 3) D1[tau] = __builtin_amdgcn_mfma_f32_32x32x16_bf16(wf[g % 3], xb, D1[tau], 0, 0, 0);
    else D2[tau] = __builtin_amdgcn_mfma_f32_32x32x16_bf16(wf[g % 3], xb, D2[tau], 0, 0, 0);
    if (a < 3 && i < 8) mix1(a + 1, i, X[(a + 1) & 1][0], X[(a + 1) & 1][1], X[(a + 1) & 1][2]);
  }
  __builtin_amdgcn_sched_barrier(0);
  STAMP(13);
  __syncthreads();
  {
    float* ob = (float*)(lds + LE_OUT) + (wv * 32 + me) * 68 + 4 * he;
    #pragma unroll
    for (int tau = 0; tau < 2; ++tau)
      #pragma unroll
      for (int g4 = 0; g4 < 4; ++g4) {
        f32x4 v;
        #pragma unroll
        for (int u = 0; u < 4; ++u) v[u] = (q == 0) ? D2[tau][4 * g4 + u] : D1[tau][4 * g4 + u];
        *(f32x4*)(ob + 32 * tau + 8 * g4) = v;
      }
  }
  __syncthreads();
  STAMP(14);
  if ((int)te < nn * 8) {
    const int v = te >> 3, k8 = te & 7;
    const u32 inf = ((const u32*)(lds + L_NINF))[v];
    const u32 w0_ = inf & 15, nw = inf >> 4;
    f32x4 u[4][2];
    { const u32 sl = v - ((const u32*)(lds + L_FN))[w0_];
      const float* ob = (const float*)(lds + LE_OUT) + ((w0_ * 32 + 4 * sl) & 255) * 68 + 8 * k8;
      #pragma unroll
      for (int i = 0; i < 4; ++i) { u[i][0] = *(const f32x4*)(ob + 68 * i); u[i][1] = *(const f32x4*)(ob + 68 * i + 4); }
      if (nw == 0) {
        #pragma unroll
        for (int i = 0; i < 4; ++i) { u[i][0] = f32x4{0, 0, 0, 0}; u[i][1] = f32x4{0, 0, 0, 0}; }
      } }
    for (u32 c = 1; c < nw; ++c) {
      const float* ob = (const float*)(lds + LE_OUT) + ((w0_ + c) * 32) * 68 + 8 * k8;
      #pragma unroll
      for (int i = 0; i < 4; ++i) { u[i][0] += *(const f32x4*)(ob + 68 * i); u[i][1] += *(const f32x4*)(ob + 68 * i + 4); }
    }
    float gate[8];
    #pragma unroll
    for (int i = 0; i < 4; ++i) {
      gate[i] = __builtin_amdgcn_rcpf(1.f + __builtin_amdgcn_exp2f(-1.44269504f * u[0][0][i]));
      gate[4 + i] = __builtin_amdgcn_rcpf(1.f + __builtin_amdgcn_exp2f(-1.44269504f * u[0][1][i]));
    }
    u16* op = out + (size_t)(node0 + v) * 256 + 8 * k8;
    *(u32x4*)op = u32x4{pk2(u[0][0][0], u[0][0][1]), pk2(u[0][0][2], u[0][0][3]), pk2(u[0][1][0], u[0][1][1]), pk2(u[0][1][2], u[0][1][3])};
    #pragma unroll
    for (int i = 1; i < 4; ++i)
      *(u32x4*)(op + 64 * i) = u32x4{pk2(u[i][0][0] * gate[0], u[i][0][1] * gate[1]), pk2(u[i][0][2] * gate[2], u[i][0][3] * gate[3]),
                                     pk2(u[i][1][0] * gate[4], u[i][1][1] * gate[5]), pk2(u[i][1][2] * gate[6], u[i][1][3] * gate[7])};
  }
  STAMP(15);
  STAMP_FLUSH();
}

#define FUSED 1
extern "C" void kernel_launch(void* const* d_in, const int* in_sizes, int n_in, void* d_out, int out_size, void* d_ws, size_t ws_size,
                              hipStream_t stream) {
  const u16* x = (const u16*)d_in[0]; const u16* rhat = (const u16*)d_in[1]; const u16* ea = (const u16*)d_in[2];
  const int* src = (const int*)d_in[3]; const int* dst = (const int*)d_in[4];
  const u16* wrad = (const u16*)d_in[5]; const u16* w0 = (const u16*)d_in[6]; const u16* w1 = (const u16*)d_in[7]; const u16* eps = (const u16*)d_in[8];
  const int N = in_sizes[0] / 256, E = in_sizes[3];
  const int CH = ((E + NWG - 1) / NWG + 7) & ~7;
  const u32 rmul = (u32)(((u64)NR << 32) / (u32)N);
  char* ws = (char*)d_ws;
#define EXTRA
#define EXTRB
  if (FUSED) {
    (void)hipFuncSetAttribute((const void*)k_main<0>, hipFuncAttributeMaxDynamicSharedMemorySize, L_TOTAL);
    k_main<0><<<NWG, 512, L_TOTAL, stream>>>(x, rhat, ea, src, dst, wrad, w0, w1, eps, (u16*)d_out, ws, N, E, CH, rmul EXTRA);
  } else {
    (void)hipFuncSetAttribute((const void*)k_main<1>, hipFuncAttributeMaxDynamicSharedMemorySize, L_TOTAL);
    (void)hipFuncSetAttribute((const void*)k_main<2>, hipFuncAttributeMaxDynamicSharedMemorySize, L_TOTAL);
    k_main<1><<<NWG, 512, L_TOTAL, stream>>>(x, rhat, ea, src, dst, wrad, w0, w1, eps, (u16*)d_out, ws, N, E, CH, rmul EXTRA);
    k_main<2><<<NWG, 512, L_TOTAL, stream>>>(x, rhat, ea, src, dst, wrad, w0, w1, eps, (u16*)d_out, ws, N, E, CH, rmul EXTRB);
  }
}
```

```cpp
#include <hip/hip_runtime.h>
#include <stdint.h>

typedef unsigned short u16;
typedef unsigned u32;
typedef unsigned long long u64;
typedef __attribute__((ext_vector_type(4))) unsigned u32x4;
typedef __attribute__((ext_vector_type(2))) unsigned u32x2;
typedef __attribute__((ext_vector_type(4))) float f32x4;
typedef __attribute__((ext_vector_type(16))) float f32x16;
typedef __attribute__((ext_vector_type(8))) __bf16 bf16x8;
typedef __attribute__((ext_vector_type(4))) short s16x4;
typedef __attribute__((ext_vector_type(2))) __bf16 bf16x2;
#define LDSP(T) __attribute__((address_space(3))) T*
#define GASP(T) __attribute__((address_space(1))) T*

#define STAMP(i)
#define STAMP_FLUSH()
#define PT_STRIDE 65536
#define PRIO 1
#define NPF 0

constexpr int NB = 49;
constexpr int NWG = 256;
constexpr int CHMAX = 3136;
constexpr int CAP = 4096;
constexpr int NR = 16;
constexpr int NSLOT = CAP + 256;

constexpr int L_W = 0;
constexpr int L_R = 40960;
constexpr int LA_CNT = L_R;
constexpr int LA_KEY = LA_CNT + 8192;
constexpr int LA_PAY = LA_KEY + CHMAX * 4;
constexpr int LA_TOT = LA_PAY + CHMAX * 8;
constexpr int LA_END = LA_TOT + 16;
constexpr int LB_KEY = L_R;
constexpr int LB_PAY = LB_KEY + NSLOT * 4;
constexpr int LB_MARK = LB_PAY + NSLOT * 8;
constexpr int LB_NCNT = LB_MARK + 8 * 64;
constexpr int LB_CNT = LB_NCNT + 2048 + 16;
constexpr int LB_TAB = LB_CNT + 4096 + 16;
constexpr int LB_END = LB_TAB + 4096;
constexpr int LE_OUT = L_R;
constexpr int LE_END = LE_OUT + 8 * 32 * 68 * 4;
constexpr int cmax(int a, int b) { return a > b ? a : b; }
constexpr int L_ZERO = cmax(cmax(LA_END, LB_END), LE_END);
constexpr int L_MISC = L_ZERO + 16;
constexpr int L_KAP = L_MISC + 64;
constexpr int L_NINF = L_KAP + 192;
constexpr int L_FN = L_NINF + 256;
constexpr int L_TOTAL = L_FN + 32;
constexpr u32 PADKEY = 0x7FFFFE00u | 63u;
constexpr u32 OOB = 0x7ffffff0u;

constexpr size_t W_EPOCH = 0;
constexpr size_t W_GRAN = 256;
constexpr size_t W_KEY = W_GRAN + (size_t)NWG * NWG * 8;
constexpr size_t W_PAY = W_KEY + (size_t)NWG * CHMAX * 4;

__device__ __forceinline__ float bf2f(u32 v) { return __uint_as_float(v << 16); }
__device__ __forceinline__ float bfhi(u32 v) { return __uint_as_float(v & 0xffff0000u); }
__device__ __forceinline__ u32 pk2(float lo, float hi) {
  bf16x2 v; v[0] = (__bf16)lo; v[1] = (__bf16)hi; return __builtin_bit_cast(u32, v);
}
__device__ __forceinline__ void opaque(u32& v) { asm volatile("" : "+v"(v)); }
__device__ __forceinline__ u32 wave_incl_scan(u32 v) {
  v += (u32)__builtin_amdgcn_update_dpp(0, (int)v, 0x111, 0xf, 0xf, true);
  v += (u32)__builtin_amdgcn_update_dpp(0, (int)v, 0x112, 0xf, 0xf, true);
  v += (u32)__builtin_amdgcn_update_dpp(0, (int)v, 0x114, 0xf, 0xf, true);
  v += (u32)__builtin_amdgcn_update_dpp(0, (int)v, 0x118, 0xf, 0xf, true);
  v += (u32)__builtin_amdgcn_update_dpp(0, (int)v, 0x142, 0xa, 0xf, false);
  v += (u32)__builtin_amdgcn_update_dpp(0, (int)v, 0x143, 0xc, 0xf, false);
  return v;
}
#define DPPOP(name, qp) \
  __device__ __forceinline__ float name##_mul(float s, float k) { float d; asm("v_mul_f32_dpp %0, %1, %2 quad_perm:" qp " row_mask:0xf bank_mask:0xf" : "=v"(d) : "v"(s), "v"(k)); return d; } \
  __device__ __forceinline__ void name##_fma(float& d, float s, float k) { asm("v_fmac_f32_dpp %0, %1, %2 quad_perm:" qp " row_mask:0xf bank_mask:0xf" : "+v"(d) : "v"(s), "v"(k)); }
DPPOP(q0, "[0,0,0,0]") DPPOP(q1, "[1,1,1,1]") DPPOP(q2, "[2,2,2,2]") DPPOP(q3, "[3,3,3,3]")

#define RSRC(p, bytes) __builtin_amdgcn_make_buffer_rsrc((void*)(p), 0, (bytes), 0x00020000)
constexpr int SC1 = 16;

template <int MODE>
__global__ __launch_bounds__(512, 2) void k_main(const u16* __restrict__ x, const u16* __restrict__ rhat, const u16* __restrict__ ea,
                                                 const int* __restrict__ src, const int* __restrict__ dst, const u16* __restrict__ wrad,
                                                 const u16* __restrict__ w0, const u16* __restrict__ w1, const u16* __restrict__ eps,
                                                 u16* __restrict__ out, char* __restrict__ ws, int N, int E, int CH, u32 rmul
                                                 ) {
  extern __shared__ __attribute__((aligned(16))) char lds[];
  const int t = threadIdx.x, lane = t & 63, wv = __builtin_amdgcn_readfirstlane(t >> 6);
  const int xcd = blockIdx.x & 7, rank = blockIdx.x >> 3;
  const u32 ldsbase = (u32)(size_t)lds;
  constexpr int AUX = MODE == 0 ? SC1 : 0;
  STAMP(0);
  const __amdgpu_buffer_rsrc_t rkey = RSRC(ws + W_KEY, NWG * CH * 4), rpay = RSRC(ws + W_PAY, NWG * CH * 8);

  const int p = blockIdx.x;
  const int nval = min(CH, E - p * CH);
  const int nmine = min(8, nval - 8 * t);
  u32x4 dv[2], sv[2], av, rv[3];
  if (MODE != 2) {
    const u32 e0 = p * CH + 8 * t;
    const bool any = nmine > 0;
    const __amdgpu_buffer_rsrc_t rd = RSRC(dst, E * 4), rsr = RSRC(src, E * 4), ra = RSRC(ea, (E * 2 + 3) & ~3), rr = RSRC(rhat, (E * 6 + 3) & ~3);
    const u32 o4 = any ? e0 * 4 : OOB, o2 = any ? e0 * 2 : OOB, o6 = any ? e0 * 6 : OOB;
    dv[0] = __builtin_amdgcn_raw_buffer_load_b128(rd, o4, 0, 0); dv[1] = __builtin_amdgcn_raw_buffer_load_b128(rd, o4 + 16, 0, 0);
    sv[0] = __builtin_amdgcn_raw_buffer_load_b128(rsr, o4, 0, 0); sv[1] = __builtin_amdgcn_raw_buffer_load_b128(rsr, o4 + 16, 0, 0);
    av = __builtin_amdgcn_raw_buffer_load_b128(ra, o2, 0, 0);
    rv[0] = __builtin_amdgcn_raw_buffer_load_b128(rr, o6, 0, 0); rv[1] = __builtin_amdgcn_raw_buffer_load_b128(rr, o6 + 16, 0, 0);
    rv[2] = __builtin_amdgcn_raw_buffer_load_b128(rr, o6 + 32, 0, 0);
    __builtin_amdgcn_sched_barrier(0);
  }
  u32 tch = 0;
  if (MODE == 0 && wv == 7) {
    const u32 nx = ((u32)N * 512 + PT_STRIDE - 1) / PT_STRIDE, nw_ = (u32)((W_PAY + (size_t)NWG * CHMAX * 8 + PT_STRIDE - 1) / PT_STRIDE);
    for (u32 i = rank * 64 + lane; i < 2 * nx + nw_; i += 32 * 64) {
      const char* pp = i < nx ? (const char*)x + (size_t)i * PT_STRIDE : i < 2 * nx ? (const char*)out + (size_t)(i - nx) * PT_STRIDE : ws + (size_t)(i - 2 * nx) * PT_STRIDE;
      tch ^= *(const volatile u32*)pp;
    }
  }
  u64 ep = 0;
  if (MODE == 0 && t == 511) ep = __hip_atomic_load((GASP(u64))(ws + W_EPOCH), __ATOMIC_RELAXED, __HIP_MEMORY_SCOPE_AGENT);
  u32x4 wr[10]; u32 wsc[10]; u32 pf[4] = {0, 0, 0, 0}; u32 epsv = 0;
  const int u4 = t - 256;
  auto side_loads = [&]() {
    epsv = eps[(u4 >= 9 && u4 < 36) ? u4 - 9 : 0];
    #pragma unroll
    for (int i = 0; i < 10; ++i) {
      const int r = (u4 >> 3) + 32 * i;
      const u16* wp = i < 6 ? w1 + r * 64 : w0 + (r - 192) * 64;
      wr[i] = *(const u32x4*)(wp + 8 * (u4 & 7));
      wsc[i] = wrad[i < 6 ? r + 128 : r - 192];
    }
    __builtin_amdgcn_sched_barrier(0);
    const u32 xb = (u32)N * 512, st0 = ((u32)xcd * N >> 3) * 512;
    #pragma unroll
    for (int i = 0; i < 4; ++i) {
      const u32 li = u4 + 256 * i;
      u32 o = st0 + (rank * NPF + li) * 128; o = o >= xb ? o - xb : o;
      if (256 * i < NPF) pf[i] = __builtin_amdgcn_raw_buffer_load_b32(RSRC(x, xb), li < NPF ? o : OOB, 0, 0);
    }
    __builtin_amdgcn_sched_barrier(0);
  };
  auto weights_to_lds = [&]() {
    #pragma unroll
    for (int i = 0; i < 10; ++i) {
      const int r = (u4 >> 3) + 32 * i;
      opaque(wsc[i]);
      const float s = bf2f(wsc[i]);
      u32x4 v;
      #pragma unroll
      for (int j = 0; j < 4; ++j) v[j] = pk2(s * bf2f(wr[i][j]), s * bfhi(wr[i][j]));
      *(u32x4*)(lds + L_W + r * 128 + (((u4 & 7) ^ (((r >> 1) & 1) << 2)) << 4)) = v;
    }
    opaque(epsv);
    if (u4 < 36) {
      const int qq = u4 / 9, jk = u4 - 9 * qq;
      ((float*)(lds + L_KAP))[qq * 12 + jk] = qq == 0 ? ((jk == 0 || jk == 4 || jk == 8) ? 1.f : 0.f) : bf2f(epsv);
    }
    asm volatile("" :: "v"(pf[0]), "v"(pf[1]), "v"(pf[2]), "v"(pf[3]));
    if (u4 == 0) *(u32x4*)(lds + L_ZERO) = u32x4{0, 0, 0, 0};
  };
  if (MODE == 2 && wv >= 4) side_loads();

  u32 gval = 0; u64 tag = 0;
  if (MODE != 2) {
    u32* cnt = (u32*)(lds + LA_CNT); u32* skey = (u32*)(lds + LA_KEY); u32x2* spay = (u32x2*)(lds + LA_PAY); u32* wtot = (u32*)(lds + LA_TOT);
    *(u32x4*)&cnt[4 * t] = u32x4{0, 0, 0, 0};
    __syncthreads();
    STAMP(1);
    u32 ci[8], rk[8], nl[8];
    #pragma unroll
    for (int i = 0; i < 8; ++i) {
      const u32 d = dv[i >> 2][i & 3];
      const u32 b = d / NB;
      nl[i] = d - b * NB;
      ci[i] = b * 8 + (wv ^ ((b >> 2) & 7));
      rk[i] = 0;
      if (i < nmine) rk[i] = atomicAdd(&cnt[ci[i]], 1u);
    }
    u32 c01[8], c23[8];
    #pragma unroll
    for (int i = 0; i < 8; ++i) {
      const u32 aw = av[i >> 1]; const float a = (i & 1) ? bfhi(aw) : bf2f(aw);
      float y[3];
      #pragma unroll
      for (int j = 0; j < 3; ++j) { const int id = 3 * i + j; const u32 w = rv[id >> 3][(id >> 1) & 3]; y[j] = (id & 1) ? bfhi(w) : bf2f(w); }
      c01[i] = pk2(a, a * y[0]); c23[i] = pk2(a * y[1], a * y[2]);
      opaque(c01[i]); opaque(c23[i]);
    }
    u32 kk[8];
    #pragma unroll
    for (int i = 0; i < 8; ++i) { kk[i] = (sv[i >> 2][i & 3] << 9) | nl[i]; opaque(kk[i]); }
    asm volatile("" :: "v"(tch));
    if (MODE == 0 && t == 511) *(u64*)(lds + L_MISC + 32) = (ep + 1) & 0xffffffffffull;
    __syncthreads();
    STAMP(2);
    u32x4 ca, cb; u32 tot = 0, incl = 0;
    if (MODE == 0 && wv >= 4) side_loads();
    if (t < NWG) {
      ca = *(const u32x4*)&cnt[8 * t]; cb = *(const u32x4*)&cnt[8 * t + 4];
      u32 run = 0, v;
      #pragma unroll
      for (int i = 0; i < 4; ++i) { v = ca[i]; ca[i] = run; run += v; }
      #pragma unroll
      for (int i = 0; i < 4; ++i) { v = cb[i]; cb[i] = run; run += v; }
      tot = run; incl = wave_incl_scan(tot);
      if (lane == 63) wtot[wv] = incl;
    }
    __syncthreads();
    if (t < NWG) {
      const u32x4 wt = *(const u32x4*)wtot;
      const u32 base = incl - tot + (wv > 0 ? wt[0] : 0) + (wv > 1 ? wt[1] : 0) + (wv > 2 ? wt[2] : 0);
      *(u32x4*)&cnt[8 * t] = ca + base; *(u32x4*)&cnt[8 * t + 4] = cb + base;
      gval = base | ((base + tot) << 12);
    }
    __syncthreads();
    STAMP(3);
    #pragma unroll
    for (int i = 0; i < 8; ++i) {
      u32 pos = cnt[ci[i]] + rk[i]; opaque(pos);
      pos = (i < nmine) ? pos : CHMAX - 1;
      skey[pos] = kk[i];
      spay[pos] = u32x2{c01[i], c23[i]};
    }
    __syncthreads();
    STAMP(4);
    {
      const u32 nk = (nval > 0 ? nval : 0) * 4, np = nk * 2;
      #pragma unroll
      for (int i = 0; i < 2; ++i) {
        const u32 o = (t + 512 * i) * 16;
        const u32x4 v = *(const u32x4*)(lds + LA_KEY + (o < CHMAX * 4 ? o : 0));
        __builtin_amdgcn_raw_buffer_store_b128(v, rkey, o < nk ? p * CH * 4 + o : OOB, 0, AUX);
      }
      #pragma unroll
      for (int i = 0; i < 4; ++i) {
        const u32 o = (t + 512 * i) * 16;
        const u32x4 v = *(const u32x4*)(lds + LA_PAY + (o < CHMAX * 8 ? o : 0));
        __builtin_amdgcn_raw_buffer_store_b128(v, rpay, o < np ? p * CH * 8 + o : OOB, 0, AUX);
      }
    }
    STAMP(5);
    if (MODE == 0) {
      __builtin_amdgcn_sched_barrier(0);
      if (wv >= 4) weights_to_lds();
      asm volatile("s_waitcnt vmcnt(0)" ::: "memory");
      __syncthreads();
    }
    if (MODE == 0) tag = *(const u64*)(lds + L_MISC + 32);
    if (t < NWG) {
      GASP(u64) g = (GASP(u64))(ws + W_GRAN) + (size_t)t * NWG + p;
      if (MODE == 0) __hip_atomic_store(g, (tag << 24) | gval, __ATOMIC_RELAXED, __HIP_MEMORY_SCOPE_AGENT);
      else *g = gval;
    }
    STAMP(6);
  }
  if (MODE == 1) { STAMP_FLUSH(); return; }

  if (MODE == 2 && wv >= 4) weights_to_lds();

  const int bkt = xcd * 32 + rank;
  const int node0 = bkt * NB;
  const int nn = min(NB, N - node0);
  u32* keys = (u32*)(lds + LB_KEY); u32x2* pay = (u32x2*)(lds + LB_PAY);
  u32* mark = (u32*)(lds + LB_MARK) + wv * 16;
  u32* ncnt = (u32*)(lds + LB_NCNT); u32* cntb = (u32*)(lds + LB_CNT); u32* tab = (u32*)(lds + LB_TAB) + wv * 128;
  u32* misc = (u32*)(lds + L_MISC);

  f32x16 acc[8];
  #pragma unroll
  for (int j = 0; j < 8; ++j)
    #pragma unroll
    for (int r = 0; r < 16; ++r) acc[j][r] = 0.f;
  const int h = lane >> 5, m = lane & 31;
  const __amdgpu_buffer_rsrc_t rs = RSRC(x, N * 512);
  const u32 moff = m * 16;
  const u32 rot = 2 * xcd;
  const int sup_r = (lane >> 2) & 3;
  const u32 myslot = 4 * ((lane >> 4) & 1) + (lane & 3);
  u32* ninf = (u32*)(lds + L_NINF); u32* fnl = (u32*)(lds + L_FN);
  auto clear_round = [&]() {
    if (lane < 16) mark[lane] = 0;
    cntb[t] = 0; cntb[t + 512] = 0; ncnt[t] = 0;
  };
  clear_round();
  asm volatile("" : "+v"(acc[0]), "+v"(acc[1]), "+v"(acc[2]), "+v"(acc[3]), "+v"(acc[4]), "+v"(acc[5]), "+v"(acc[6]), "+v"(acc[7]));
  __builtin_amdgcn_sched_barrier(0);

  const int sp = 32 * wv + (lane & 31);
  u32 o;
  {
    GASP(u64) g = (GASP(u64))(ws + W_GRAN) + (size_t)bkt * NWG + sp;
    if (MODE == 0) {
      u64 v; u32 spins = 0;
      for (;;) {
        v = __hip_atomic_load(g, __ATOMIC_RELAXED, __HIP_MEMORY_SCOPE_AGENT);
        if (__all((v >> 24) == tag)) break;
        if (++spins > (1u << 17)) break;
        __builtin_amdgcn_s_sleep(1);
      }
      o = (u32)v & 0xffffffu;
    } else o = (u32)*g & 0xffffffu;
  }
  STAMP(7);
  const u32 sstart = o & 0xfff;
  const u32 slen = (lane < 32) ? (o >> 12) - sstart : 0u;
  const u32 sincl = wave_incl_scan(slen);
  const u32 spre = sincl - slen;
  const u32 cntw = __builtin_amdgcn_readlane(sincl, 63);
  const u64 nem = __ballot(slen != 0);
  const u32 nrk = __builtin_amdgcn_mbcnt_hi((u32)(nem >> 32), __builtin_amdgcn_mbcnt_lo((u32)nem, 0));
  const u32 sG = (u32)__builtin_amdgcn_ds_permute((int)((slen != 0 ? nrk : 63u) << 2), (int)((u32)sp * CH + sstart - spre));
  if (lane == 0) misc[wv] = cntw;

  u32 r0 = 0, maxw;
  do {
    { const u32 pb = spre - 1 - r0;
      if (slen != 0 && spre != 0 && pb < 512) atomicOr(&mark[pb >> 5], 1u << (pb & 31)); }
    u32 carry = __builtin_popcountll(__ballot(slen != 0 && spre != 0 && spre <= r0));
    u32x4 mk[4];
    #pragma unroll
    for (int i = 0; i < 4; ++i) mk[i] = *(const u32x4*)&mark[4 * i];
    u32 gi[8];
    #pragma unroll
    for (int i = 0; i < 8; ++i) {
      const u32 lo = mk[i >> 1][2 * (i & 1)], hi = mk[i >> 1][2 * (i & 1) + 1];
      const u32 rkk = __builtin_amdgcn_mbcnt_hi(hi, __builtin_amdgcn_mbcnt_lo(lo, carry));
      carry += __builtin_popcount(lo) + __builtin_popcount(hi);
      gi[i] = (u32)__builtin_amdgcn_ds_bpermute((int)(rkk << 2), (int)sG);
    }
    __builtin_amdgcn_sched_barrier(0);
    u32 k[8], br[8]; u32x2 py[8];
    #pragma unroll
    for (int i = 0; i < 8; ++i) {
      const u32 r = r0 + lane + 64 * i;
      u32 g = gi[i] + r; opaque(g);
      const bool ok = r < cntw;
      k[i] = __builtin_amdgcn_raw_buffer_load_b32(rkey, ok ? g * 4 : OOB, 0, AUX);
      py[i] = __builtin_amdgcn_raw_buffer_load_b64(rpay, ok ? g * 8 : OOB, 0, AUX);
    }
    STAMP(8);
    __syncthreads();
    { const u32x4 m0 = *(const u32x4*)misc, m1 = *(const u32x4*)(misc + 4);
      maxw = __builtin_amdgcn_readfirstlane(max(max(max(m0[0], m0[1]), max(m0[2], m0[3])), max(max(m1[0], m1[1]), max(m1[2], m1[3])))); }
    if (MODE == 0 && blockIdx.x == 0 && t == 0 && r0 == 0) {
      __hip_atomic_store((GASP(u64))(ws + W_EPOCH), tag, __ATOMIC_RELAXED, __HIP_MEMORY_SCOPE_AGENT);
    }
    bool split = maxw <= 512;
    u32 cw[8]; u32 fnv = 0, ninfo = 0;
    if (split) {
      u32 rk1[8];
      #pragma unroll
      for (int i = 0; i < 8; ++i) {
        u32 ix = wv * 64 + (k[i] & 63); opaque(ix);
        ix = (r0 + lane + 64 * i < cntw) ? ix : 512;
        rk1[i] = atomicAdd(&ncnt[ix], 1u);
      }
      __syncthreads();
      STAMP(9);
      u32 c[8];
      #pragma unroll
      for (int w = 0; w < 8; ++w) c[w] = ncnt[w * 64 + lane];
      u32 tot = 0, pre = 0;
      #pragma unroll
      for (int w = 0; w < 8; ++w) { pre = (w == wv) ? tot : pre; tot += c[w]; }
      const u32 S = wave_incl_scan(tot);
      const u32 ns = S - tot, nb = ns + pre;
      const u32 total = __builtin_amdgcn_readlane(S, 63);
      const u32 L = total ? ((total + 127) >> 7) << 4 : 16;
      const float invL = __builtin_amdgcn_rcpf((float)L);
      const u32 wf = (u32)(((float)ns + 0.5f) * invL);
      const u32 wl = (u32)(((float)S - 0.5f) * invL);
      const u32 gl = (wf * L == ns) ? wf : wf + 1;
      const bool has = tot != 0 && gl * L < S;
      fnv = (u32)__builtin_amdgcn_ds_permute((int)((has ? gl : 63u) << 2), (int)lane);
      fnv = (lane * L < total) ? fnv : 64u;
      const u32 f0 = (u32)__builtin_amdgcn_ds_bpermute((int)(wf << 2), (int)fnv);
      split = !__any(tot != 0 && (wl > gl || lane - f0 > 7));
      ninfo = tot ? (wf | ((wl - wf + 1) << 4)) : 0u;
      if (split) {
        #pragma unroll
        for (int i = 0; i < 8; ++i) rk1[i] += (u32)__builtin_amdgcn_ds_bpermute((int)((k[i] & 63) << 2), (int)nb);
        #pragma unroll
        for (int i = 0; i < 8; ++i) cw[i] = (u32)(((float)rk1[i] + 0.5f) * invL);
      }
    }
    if (!split) {
      fnv = 7 * lane; ninfo = ((lane * 37) >> 8) | 16u;
      #pragma unroll
      for (int i = 0; i < 8; ++i) cw[i] = ((k[i] & 63) * 37) >> 8;
    }
    if (wv == 0) { ninf[lane] = ninfo; if (lane < 8) fnl[lane] = fnv; }
    const u32 myfn = __builtin_amdgcn_readlane(fnv, wv);
    #pragma unroll
    for (int i = 0; i < 8; ++i) {
      u32 bin = cw[i] * NR + ((__umulhi(k[i] >> 9, rmul) - rot) & (NR - 1));
      opaque(bin);
      bin = (r0 + lane + 64 * i < cntw) ? bin : 1024 - wv * 128;
      br[i] = (bin << 16) | atomicAdd(&cntb[wv * 128 + bin], 1u);
    }
    __syncthreads();
    STAMP(10);
    u32 mylb, mycnt;
    {
      u32x2 c[8];
      #pragma unroll
      for (int w = 0; w < 8; ++w) c[w] = *(const u32x2*)&cntb[w * 128 + 2 * lane];
      u32 t0 = 0, t1 = 0, p0 = 0, p1 = 0;
      #pragma unroll
      for (int w = 0; w < 8; ++w) { p0 = (w == wv) ? t0 : p0; p1 = (w == wv) ? t1 : p1; t0 += c[w][0]; t1 += c[w][1]; }
      const u32 ps = t0 + t1;
      const u32 S = wave_incl_scan(ps);
      const u32 send = (u32)__builtin_amdgcn_ds_bpermute((lane | 7) << 2, (int)S);
      const u32 gstart = (u32)__builtin_amdgcn_ds_bpermute((lane & ~7) << 2, (int)(S - ps));
      const u32 len = send - gstart;
      const u32 padl = ((lane & 7) == 7) ? ((len + 15) & ~15u) + 16 - len : 0u;
      const u32 P = wave_incl_scan(padl) - padl;
      const u32 padprev = (u32)__builtin_amdgcn_ds_bpermute((lane & ~7) << 2, (int)P);
      const u32 b0 = S - ps + padprev;
      *(u32x2*)&tab[2 * lane] = u32x2{b0 + p0, b0 + t0 + p1};
      mylb = __builtin_amdgcn_readlane(gstart + padprev, 8 * wv);
      mycnt = __builtin_amdgcn_readlane(len, 8 * wv);
    }
    #pragma unroll
    for (int i = 0; i < 8; ++i) br[i] = tab[(br[i] >> 16) & 127] + (br[i] & 0xffff);
    #pragma unroll
    for (int i = 0; i < 8; ++i) {
      u32 pos = br[i]; opaque(pos);
      pos = (r0 + lane + 64 * i < cntw) ? pos : NSLOT - 1;
      keys[pos] = k[i]; pay[pos] = py[i];
    }
    const u32 nsteps = (mycnt + 15) >> 4;
    if (lane < 32) { const u32 i = mycnt + lane; if (i < nsteps * 16 + 16) { keys[mylb + i] = PADKEY; pay[mylb + i] = u32x2{0, 0}; } }
    __syncthreads();
    STAMP(11);

    if (nsteps > 0) {
      const u32 mynode = myfn + myslot;
      const u32* kp = keys + mylb + 8 * h;
      const u32* ks = keys + mylb + 8 * h + sup_r;
      const u32 payaddr = ldsbase + LB_PAY + (mylb + 8 * h + sup_r) * 8;
      const u32 zaddr = ldsbase + L_ZERO;
      u32x4 R[8];
      {
        const u32x4 ka = *(const u32x4*)kp, kb = *(const u32x4*)(kp + 4);
        #pragma unroll
        for (int i = 0; i < 4; ++i) R[i] = __builtin_amdgcn_raw_buffer_load_b128(rs, (ka[i] & ~511u) | moff, 0, 0);
        #pragma unroll
        for (int i = 0; i < 4; ++i) R[4 + i] = __builtin_amdgcn_raw_buffer_load_b128(rs, (kb[i] & ~511u) | moff, 0, 0);
      }
      for (u32 s = 0; s < nsteps; ++s) {
        if (PRIO == 3) __builtin_amdgcn_s_setprio(3);
        if (PRIO == 1) { if (((s + (wv >> 2)) & 1) != 0) __builtin_amdgcn_s_setprio(1); else __builtin_amdgcn_s_setprio(0); }
        if (PRIO == 2) { if (s == 0 && wv >= 4) __builtin_amdgcn_s_setprio(1); }
        const u32x4 ka = *(const u32x4*)(kp + 16 * (s + 1)), kb = *(const u32x4*)(kp + 16 * (s + 1) + 4);
        const u32 k0 = ks[16 * s], k1 = ks[16 * s + 4];
        const u32 a0 = ((k0 & 63) == mynode) ? payaddr + s * 128 : zaddr;
        const u32 a1 = ((k1 & 63) == mynode) ? payaddr + s * 128 + 32 : zaddr;
        const s16x4 c0 = __builtin_amdgcn_ds_read_tr16_b64_v4i16((LDSP(s16x4))(uintptr_t)a0);
        const s16x4 c1 = __builtin_amdgcn_ds_read_tr16_b64_v4i16((LDSP(s16x4))(uintptr_t)a1);
        const bf16x8 cf = __builtin_bit_cast(bf16x8, __builtin_shufflevector(c0, c1, 0, 1, 2, 3, 4, 5, 6, 7));
        u32x4 F[8];
        #pragma unroll
        for (int dd = 0; dd < 4; ++dd) {
          #pragma unroll
          for (int j = 0; j < 8; ++j)
            F[j][dd] = __builtin_amdgcn_perm(R[2 * dd + 1][j >> 1], R[2 * dd][j >> 1], (j & 1) ? 0x07060302u : 0x05040100u);
          __builtin_amdgcn_sched_barrier(0);
          const u32 ke = dd < 2 ? ka[2 * dd] : kb[2 * dd - 4], ko = dd < 2 ? ka[2 * dd + 1] : kb[2 * dd - 3];
          R[2 * dd] = __builtin_amdgcn_raw_buffer_load_b128(rs, (ke & ~511u) | moff, 0, 0);
          R[2 * dd + 1] = __builtin_amdgcn_raw_buffer_load_b128(rs, (ko & ~511u) | moff, 0, 0);
          __builtin_amdgcn_sched_barrier(0);
        }
        if (PRIO == 3) __builtin_amdgcn_s_setprio(0);
        #pragma unroll
        for (int j = 0; j < 8; ++j) acc[j] = __builtin_amdgcn_mfma_f32_32x32x16_bf16(__builtin_bit_cast(bf16x8, F[j]), cf, acc[j], 0, 0, 0);
        __builtin_amdgcn_sched_barrier(0);
      }
    }
    r0 += 512;
    if (r0 < maxw) { __syncthreads(); clear_round(); }
  } while (r0 < maxw);
  if (PRIO) __builtin_amdgcn_s_setprio(0);
  STAMP(12);

  u32 te = t; opaque(te);
  const int q = te & 3;
  float kap[9];
  { const float* kl = (const float*)(lds + L_KAP) + q * 12;
    const f32x4 k0 = *(const f32x4*)kl, k1 = *(const f32x4*)(kl + 4); kap[8] = kl[8];
    #pragma unroll
    for (int i = 0; i < 4; ++i) { kap[i] = k0[i]; kap[4 + i] = k1[i]; } }
  const float dl1 = q == 1 ? 1.f : 0.f, dl2 = q == 2 ? 1.f : 0.f, dl3 = q == 3 ? 1.f : 0.f;
  asm volatile("s_nop 15\n\ts_nop 15" : "+v"(acc[0]), "+v"(acc[1]), "+v"(acc[2]), "+v"(acc[3]), "+v"(acc[4]), "+v"(acc[5]), "+v"(acc[6]), "+v"(acc[7]));
  f32x16 D1[2], D2[2];
  #pragma unroll
  for (int i = 0; i < 2; ++i)
    #pragma unroll
    for (int r = 0; r < 16; ++r) { D1[i][r] = 0.f; D2[i][r] = 0.f; }
  const u32 he = (te >> 5) & 1, me = te & 31;
  const u32 wrow = 32 * he + ((te >> 2) & 3);
  const u32 wa0 = ldsbase + L_W + wrow * 128 + 32 * ((te >> 4) & 1) + 8 * (te & 3) + ((wrow >> 1) & 1) * 64;
  const u32 wa1 = wa0 ^ 64;
  auto wfrag = [&](int g) {
    const int a = g / 10, tau = (g % 10) / 5, kind = g % 5;
    const u32 aa = (tau ? wa1 : wa0) + (64 * kind + 8 * a) * 128;
    const s16x4 lo = __builtin_amdgcn_ds_read_tr16_b64_v4i16((LDSP(s16x4))(uintptr_t)aa);
    const s16x4 hi = __builtin_amdgcn_ds_read_tr16_b64_v4i16((LDSP(s16x4))(uintptr_t)(aa + 512));
    return __builtin_bit_cast(bf16x8, __builtin_shufflevector(lo, hi, 0, 1, 2, 3, 4, 5, 6, 7));
  };
  float vb[2], vc[2];
  auto mix1 = [&](int a, int j, u32x4& Xa, u32x4& Xb, u32x4& Xc) {
    const float t1 = acc[j][4 + a], t2 = acc[j][8 + a], t3 = acc[j][12 + a];
    float bb, c;
    #define QP(n) " quad_perm:[" #n "," #n "," #n "," #n "] row_mask:0xf bank_mask:0xf\n\t"
    asm("v_mul_f32_dpp %0, %2, %5" QP(0) "v_mul_f32_dpp %1, %2, %8" QP(1)
        "v_fmac_f32_dpp %0, %3, %6" QP(0) "v_fmac_f32_dpp %1, %3, %9" QP(1)
        "v_fmac_f32_dpp %0, %4, %7" QP(0) "v_fmac_f32_dpp %1, %4, %10" QP(1)
        "v_fmac_f32_dpp %1, %2, %11" QP(2) "v_fmac_f32_dpp %1, %3, %12" QP(2) "v_fmac_f32_dpp %1, %4, %13" QP(2)
        "v_fmac_f32_dpp %1, %2, %14" QP(3) "v_fmac_f32_dpp %1, %3, %15" QP(3) "v_fmac_f32_dpp %1, %4, %16" QP(3)
        : "=&v"(bb), "=&v"(c)
        : "v"(t1), "v"(t2), "v"(t3), "v"(dl1), "v"(dl2), "v"(dl3), "v"(kap[0]), "v"(kap[1]), "v"(kap[2]), "v"(kap[3]), "v"(kap[4]), "v"(kap[5]),
          "v"(kap[6]), "v"(kap[7]), "v"(kap[8]));
    #undef QP
    vb[j & 1] = bb; vc[j & 1] = c;
    if (j & 1) { Xa[j >> 1] = pk2(acc[j - 1][a], acc[j][a]); Xb[j >> 1] = pk2(vb[0], vb[1]); Xc[j >> 1] = pk2(vc[0], vc[1]); }
  };
  u32x4 X[2][3];
  bf16x8 wf[3];
  wf[0] = wfrag(0); wf[1] = wfrag(1);
  #pragma unroll
  for (int j = 0; j < 8; ++j) mix1(0, j, X[0][0], X[0][1], X[0][2]);
  #pragma unroll
  for (int g = 0; g < 40; ++g) {
    const int a = g / 10, i = g % 10, tau = i / 5, kind = i % 5;
    __builtin_amdgcn_sched_barrier(0);
    if (g + 2 < 40) wf[(g + 2) % 3] = wfrag(g + 2);
    const bf16x8 xb = __builtin_bit_cast(bf16x8, X[a & 1][kind == 1 ? 1 : (kind == 2 || kind == 4) ? 2 : 0]);
    if (kind < 3) D1[tau] = __builtin_amdgcn_mfma_f32_32x32x16_bf16(wf[g % 3], xb, D1[tau], 0, 0, 0);
    else D2[tau] = __builtin_amdgcn_mfma_f32_32x32x16_bf16(wf[g % 3], xb, D2[tau], 0, 0, 0);
    if (a < 3 && i < 8) mix1(a + 1, i, X[(a + 1) & 1][0], X[(a + 1) & 1][1], X[(a + 1) & 1][2]);
  }
  __builtin_amdgcn_sched_barrier(0);
  STAMP(13);
  __syncthreads();
  {
    float* ob = (float*)(lds + LE_OUT) + (wv * 32 + me) * 68 + 4 * he;
    #pragma unroll
    for (int tau = 0; tau < 2; ++tau)
      #pragma unroll
      for (int g4 = 0; g4 < 4; ++g4) {
        f32x4 v;
        #pragma unroll
        for (int u = 0; u < 4; ++u) v[u] = (q == 0) ? D2[tau][4 * g4 + u] : D1[tau][4 * g4 + u];
        *(f32x4*)(ob + 32 * tau + 8 * g4) = v;
      }
  }
  __syncthreads();
  STAMP(14);
  if ((int)te < nn * 8) {
    const int v = te >> 3, k8 = te & 7;
    const u32 inf = ((const u32*)(lds + L_NINF))[v];
    const u32 w0_ = inf & 15, nw = inf >> 4;
    f32x4 u[4][2];
    { const u32 sl = v - ((const u32*)(lds + L_FN))[w0_];
      const float* ob = (const float*)(lds + LE_OUT) + ((w0_ * 32 + 4 * sl) & 255) * 68 + 8 * k8;
      #pragma unroll
      for (int i = 0; i < 4; ++i) { u[i][0] = *(const f32x4*)(ob + 68 * i); u[i][1] = *(const f32x4*)(ob + 68 * i + 4); }
      if (nw == 0) {
        #pragma unroll
        for (int i = 0; i < 4; ++i) { u[i][0] = f32x4{0, 0, 0, 0}; u[i][1] = f32x4{0, 0, 0, 0}; }
      } }
    for (u32 c = 1; c < nw; ++c) {
      const float* ob = (const float*)(lds + LE_OUT) + ((w0_ + c) * 32) * 68 + 8 * k8;
      #pragma unroll
      for (int i = 0; i < 4; ++i) { u[i][0] += *(const f32x4*)(ob + 68 * i); u[i][1] += *(const f32x4*)(ob + 68 * i + 4); }
    }
    float gate[8];
    #pragma unroll
    for (int i = 0; i < 4; ++i) {
      gate[i] = __builtin_amdgcn_rcpf(1.f + __builtin_amdgcn_exp2f(-1.44269504f * u[0][0][i]));
      gate[4 + i] = __builtin_amdgcn_rcpf(1.f + __builtin_amdgcn_exp2f(-1.44269504f * u[0][1][i]));
    }
    u16* op = out + (size_t)(node0 + v) * 256 + 8 * k8;
    *(u32x4*)op = u32x4{pk2(u[0][0][0], u[0][0][1]), pk2(u[0][0][2], u[0][0][3]), pk2(u[0][1][0], u[0][1][1]), pk2(u[0][1][2], u[0][1][3])};
    #pragma unroll
    for (int i = 1; i < 4; ++i)
      *(u32x4*)(op + 64 * i) = u32x4{pk2(u[i][0][0] * gate[0], u[i][0][1] * gate[1]), pk2(u[i][0][2] * gate[2], u[i][0][3] * gate[3]),
                                     pk2(u[i][1][0] * gate[4], u[i][1][1] * gate[5]), pk2(u[i][1][2] * gate[6], u[i][1][3] * gate[7])};
  }
  STAMP(15);
  STAMP_FLUSH();
}

#define FUSED 1
extern "C" void kernel_launch(void* const* d_in, const int* in_sizes, int n_in, void* d_out, int out_size, void* d_ws, size_t ws_size,
                              hipStream_t stream) {
  const u16* x = (const u16*)d_in[0]; const u16* rhat = (const u16*)d_in[1]; const u16* ea = (const u16*)d_in[2];
  const int* src = (const int*)d_in[3]; const int* dst = (const int*)d_in[4];
  const u16* wrad = (const u16*)d_in[5]; const u16* w0 = (const u16*)d_in[6]; const u16* w1 = (const u16*)d_in[7]; const u16* eps = (const u16*)d_in[8];
  const int N = in_sizes[0] / 256, E = in_sizes[3];
  const int CH = ((E + NWG - 1) / NWG + 7) & ~7;
  const u32 rmul = (u32)(((u64)NR << 32) / (u32)N);
  char* ws = (char*)d_ws;
#define EXTRA
#define EXTRB
  if (FUSED) {
    (void)hipFuncSetAttribute((const void*)k_main<0>, hipFuncAttributeMaxDynamicSharedMemorySize, L_TOTAL);
    k_main<0><<<NWG, 512, L_TOTAL, stream>>>(x, rhat, ea, src, dst, wrad, w0, w1, eps, (u16*)d_out, ws, N, E, CH, rmul EXTRA);
  } else {
    (void)hipFuncSetAttribute((const void*)k_main<1>, hipFuncAttributeMaxDynamicSharedMemorySize, L_TOTAL);
    (void)hipFuncSetAttribute((const void*)k_main<2>, hipFuncAttributeMaxDynamicSharedMemorySize, L_TOTAL);
    k_main<1><<<NWG, 512, L_TOTAL, stream>>>(x, rhat, ea, src, dst, wrad, w0, w1, eps, (u16*)d_out, ws, N, E, CH, rmul EXTRA);
    k_main<2><<<NWG, 512, L_TOTAL, stream>>>(x, rhat, ea, src, dst, wrad, w0, w1, eps, (u16*)d_out, ws, N, E, CH, rmul EXTRB);
  }
}
```

```cpp
#include <hip/hip_runtime.h>
#include <stdint.h>
#include <type_traits>

typedef unsigned short u16;
typedef unsigned u32;
typedef unsigned long long u64;
typedef __attribute__((ext_vector_type(4))) unsigned u32x4;
typedef __attribute__((ext_vector_type(2))) unsigned u32x2;
typedef __attribute__((ext_vector_type(4))) float f32x4;
typedef __attribute__((ext_vector_type(16))) float f32x16;
typedef __attribute__((ext_vector_type(8))) __bf16 bf16x8;
typedef __attribute__((ext_vector_type(4))) short s16x4;
typedef __attribute__((ext_vector_type(2))) __bf16 bf16x2;
#define LDSP(T) __attribute__((address_space(3))) T*
#define GASP(T) __attribute__((address_space(1))) T*


constexpr int NB = 49;
constexpr int NWG = 256;
constexpr int CHMAX = 3136;
constexpr int CAP = 4096;
constexpr int NR = 16;
constexpr int NSLOT = CAP + 256 + 64;

constexpr int L_W = 0;
constexpr int L_R = 40960;
constexpr int LA_CNT = L_R;
constexpr int LA_REC = LA_CNT + 8192 + 256;
constexpr int LA_TOT = LA_REC + CHMAX * 16;
constexpr int LA_END = LA_TOT + 16;
constexpr int LB_KEY = L_R;
constexpr int LB_PAY = LB_KEY + NSLOT * 4;
constexpr int LB_MARK = LA_END;
constexpr int LB_NCNT = LB_MARK + 8 * 64;
constexpr int LB_CNT = LB_NCNT + 2048 + 256;
constexpr int LB_TAB = LB_CNT + 4096 + 256;
constexpr int LB_END = LB_TAB + 4096;
static_assert(LB_PAY + NSLOT * 8 <= LA_END, "record lists overlap the small arrays");
constexpr int LE_OUT = L_R;
constexpr int LE_END = LE_OUT + 8 * 32 * 68 * 4;
constexpr int cmax(int a, int b) { return a > b ? a : b; }
constexpr int L_ZERO = cmax(cmax(LA_END, LB_END), LE_END);
constexpr int L_MISC = L_ZERO + 16;
constexpr int L_KAP = L_MISC + 64;
constexpr int L_NINF = L_KAP + 192;
constexpr int L_FN = L_NINF + 256;
constexpr int L_TOTAL = L_FN + 32;
constexpr u32 PADKEY = 0x7FFFFE00u | 63u;
constexpr u32 OOB = 0x7ffffff0u;

constexpr size_t W_EPOCH = 0;
constexpr size_t W_GRAN = 256;
constexpr size_t W_REC = W_GRAN + (size_t)NWG * NWG * 8;
constexpr u32 KEYMASK = 0x007FFE3Fu;

__device__ __forceinline__ float bf2f(u32 v) { return __uint_as_float(v << 16); }
__device__ __forceinline__ float bfhi(u32 v) { return __uint_as_float(v & 0xffff0000u); }
__device__ __forceinline__ u32 pk2(float lo, float hi) {
  bf16x2 v; v[0] = (__bf16)lo; v[1] = (__bf16)hi; return __builtin_bit_cast(u32, v);
}
__device__ __forceinline__ u32 mul24s(u32 v, u32 s) { u32 r; asm("v_mul_u32_u24 %0, %1, %2" : "=v"(r) : "s"(s), "v"(v)); return r; }
__device__ __forceinline__ void opaque(u32& v) { asm volatile("" : "+v"(v)); }
__device__ __forceinline__ u32 wave_incl_scan(u32 v) {
  v += (u32)__builtin_amdgcn_update_dpp(0, (int)v, 0x111, 0xf, 0xf, true);
  v += (u32)__builtin_amdgcn_update_dpp(0, (int)v, 0x112, 0xf, 0xf, true);
  v += (u32)__builtin_amdgcn_update_dpp(0, (int)v, 0x114, 0xf, 0xf, true);
  v += (u32)__builtin_amdgcn_update_dpp(0, (int)v, 0x118, 0xf, 0xf, true);
  v += (u32)__builtin_amdgcn_update_dpp(0, (int)v, 0x142, 0xa, 0xf, false);
  v += (u32)__builtin_amdgcn_update_dpp(0, (int)v, 0x143, 0xc, 0xf, false);
  return v;
}
template <int K> __device__ __forceinline__ u32 bcast8(u32 v) {
  const u32 lo = (u32)__builtin_amdgcn_update_dpp(0, (int)v, 0x150 + K, 0xf, 0x3, false);
  return (u32)__builtin_amdgcn_update_dpp((int)lo, (int)v, 0x158 + K, 0xf, 0xc, false);
}
#define RSRC(p, bytes) __builtin_amdgcn_make_buffer_rsrc((void*)(p), 0, (bytes), 0x00020000)
constexpr int SC1 = 16;

template <int MODE>
__global__ __launch_bounds__(512, 2) void k_main(const u16* __restrict__ x, const u16* __restrict__ rhat, const u16* __restrict__ ea,
                                                 const int* __restrict__ src, const int* __restrict__ dst, const u16* __restrict__ wrad,
                                                 const u16* __restrict__ w0, const u16* __restrict__ w1, const u16* __restrict__ eps,
                                                 u16* __restrict__ out, char* __restrict__ ws, int N, int E, int CH, u32 rmul) {
  extern __shared__ __attribute__((aligned(16))) char lds[];
  const int t = threadIdx.x, lane = t & 63, wv = __builtin_amdgcn_readfirstlane(t >> 6);
  const int xcd = blockIdx.x & 7, rank = blockIdx.x >> 3;
  const u32 ldsbase = (u32)(size_t)lds;
  constexpr int AUX = MODE == 0 ? SC1 : 0;
  const __amdgpu_buffer_rsrc_t rrec = RSRC(ws + W_REC, NWG * CH * 16);

  const int p = blockIdx.x;
  const int nval = min(CH, E - p * CH);
  const int nmine = min(8, nval - 8 * t);
  u32x4 dv[2], sv[2], av, rv[3]; u64 ep = 0; u32 epsv = 0, mpf = 0;
  if (MODE != 2) {
    const u32 e0 = p * CH + 8 * t;
    const bool any = nmine > 0;
    const __amdgpu_buffer_rsrc_t rd = RSRC(dst, E * 4), rsr = RSRC(src, E * 4), ra = RSRC(ea, (E * 2 + 3) & ~3), rr = RSRC(rhat, (E * 6 + 3) & ~3);
    const u32 o4 = any ? e0 * 4 : OOB, o2 = any ? e0 * 2 : OOB, o6 = any ? e0 * 6 : OOB;
    const u32 oep = (MODE == 0 && t == 447) ? 0u : OOB;
    const __amdgpu_buffer_rsrc_t rep_ = RSRC(ws + W_EPOCH, 8);
    asm volatile("s_nop 4\n\tbuffer_load_dwordx4 %0, %3, %5, 0 offen" "\n\tbuffer_load_dwordx4 %1, %3, %5, 0 offen offset:16" "\n\t"
                 "buffer_load_dwordx2 %2, %4, %6, 0 offen sc1"
                 : "=&v"(dv[0]), "=&v"(dv[1]), "=&v"(ep) : "v"(o4), "v"(oep), "s"(rd), "s"(rep_) : "memory");
    *(u32x4*)((u32*)(lds + LA_CNT) + 4 * t) = u32x4{0, 0, 0, 0};
    __syncthreads();
    const u32 oeps = (t >= 9 && t < 36) ? (t - 9) * 2 : OOB;
    const __amdgpu_buffer_rsrc_t re = RSRC(eps, 56);
    const u32 nl_ = ((u32)N * 4 + NWG - 1) / NWG;
    const u32 li = min(blockIdx.x * nl_ + min((u32)(wv * 32 + (lane & 31)), nl_ - 1), (u32)N * 4 - 1);
    const char* pfp = (const char*)x + (size_t)li * 128;
    if (wv == 0 && lane >= 32) {
      const u32 wl_ = rank * 10 + lane - 32;
      if (lane < 42) pfp = wl_ < 192 ? (const char*)w1 + wl_ * 128 : (const char*)w0 + (wl_ - 192) * 128;
      else if (lane < 47) pfp = (const char*)wrad + (lane - 42) * 128;
    }
    if (MODE != 0) pfp = (const char*)dst;
    asm volatile("s_nop 4\n\tbuffer_load_dwordx4 %0, %8, %13, 0 offen" "\n\tbuffer_load_dwordx4 %1, %8, %13, 0 offen offset:16" "\n\t"
                 "buffer_load_dwordx4 %2, %9, %14, 0 offen" "\n\t"
                 "buffer_load_dwordx4 %3, %10, %15, 0 offen" "\n\tbuffer_load_dwordx4 %4, %10, %15, 0 offen offset:16" "\n\t"
                 "buffer_load_dwordx4 %5, %10, %15, 0 offen offset:32" "\n\t"
                 "buffer_load_ushort %6, %11, %16, 0 offen\n\t"
                 "global_load_dword %7, %12, off"
                 : "=&v"(sv[0]), "=&v"(sv[1]), "=&v"(av), "=&v"(rv[0]), "=&v"(rv[1]), "=&v"(rv[2]), "=&v"(epsv), "=&v"(mpf)
                 : "v"(o4), "v"(o2), "v"(o6), "v"(oeps), "v"(pfp), "s"(rsr), "s"(ra), "s"(rr), "s"(re) : "memory");
  }
  if (MODE == 2) epsv = eps[(t >= 9 && t < 36) ? t - 9 : 0];
  auto weight_dma = [&]() {
    u32 ln = lane; opaque(ln);
    #pragma unroll
    for (int k5 = 0; k5 < 5; ++k5) {
      const int i = wv * 5 + k5;
      const u32 r = 8 * i + (ln >> 3);
      const u32 c = (ln & 7) ^ (((r >> 1) & 1) << 2);
      const u16* gp = (i < 24 ? w1 : w0 - 192 * 64) + r * 64 + 8 * c;
      const u32 dstl = ldsbase + L_W + i * 1024;
      asm volatile("s_mov_b32 m0, %0\n\ts_nop 0\n\tglobal_load_lds_dwordx4 %1, off" :: "s"(dstl), "v"(gp) : "memory");
    }
  };
  u32 wsc5[5];
  auto scale_loads = [&]() {
    u32 ln = lane; opaque(ln);
    u32 o_[5];
    #pragma unroll
    for (int k5 = 0; k5 < 5; ++k5) { const int i = wv * 5 + k5; const u32 r = 8 * i + (ln >> 3); o_[k5] = (i < 24 ? r + 128 : r - 192) * 2; }
    const __amdgpu_buffer_rsrc_t rw = RSRC(wrad, 644);
    asm volatile("s_nop 4\n\tbuffer_load_ushort %0, %5, %10, 0 offen\n\tbuffer_load_ushort %1, %6, %10, 0 offen\n\tbuffer_load_ushort %2, %7, %10, 0 offen\n\t"
                 "buffer_load_ushort %3, %8, %10, 0 offen\n\tbuffer_load_ushort %4, %9, %10, 0 offen"
                 : "=&v"(wsc5[0]), "=&v"(wsc5[1]), "=&v"(wsc5[2]), "=&v"(wsc5[3]), "=&v"(wsc5[4])
                 : "v"(o_[0]), "v"(o_[1]), "v"(o_[2]), "v"(o_[3]), "v"(o_[4]), "s"(rw) : "memory");
  };
  auto scale_weights = [&]() {
    u32 ln = lane; opaque(ln);
    u32x4* wp = (u32x4*)(lds + L_W + wv * 5120) + ln;
    #pragma unroll
    for (int k5 = 0; k5 < 5; ++k5) {
      const u32x4 wv_ = wp[64 * k5];
      opaque(wsc5[k5]);
      const float sc = bf2f(wsc5[k5]);
      u32x4 v;
      #pragma unroll
      for (int j = 0; j < 4; ++j) v[j] = pk2(sc * bf2f(wv_[j]), sc * bfhi(wv_[j]));
      wp[64 * k5] = v;
      if (k5 & 1) __builtin_amdgcn_sched_barrier(0);
    }
  };
  auto clear_small = [&]() {
    if (lane < 16) ((u32*)(lds + LB_MARK))[wv * 16 + lane] = 0;
    u32* cb_ = (u32*)(lds + LB_CNT); cb_[t] = 0; cb_[t + 512] = 0; ((u32*)(lds + LB_NCNT))[t] = 0;
  };
  u64 tag = 0, gv0 = 0;
  if (MODE != 2) {
    u32* cnt = (u32*)(lds + LA_CNT); u32x4* srec = (u32x4*)(lds + LA_REC); u32* wtot = (u32*)(lds + LA_TOT);
    asm volatile("s_waitcnt vmcnt(9)" : "+v"(dv[0]), "+v"(dv[1]) :: "memory");
    u32 ci[8], rk[8], nl[8];
    #pragma unroll
    for (int i = 0; i < 8; ++i) {
      const u32 d = dv[i >> 2][i & 3];
      u32 b = __umul24(d, 42800u) >> 21; opaque(b);
      nl[i] = (u32)(__mul24((int)b, -NB) + (int)d);
      u32 ix = b * 8 + (wv ^ ((b >> 2) & 7)); opaque(ix);
      ci[i] = (i < nmine) ? ix : 2048u + lane;
      rk[i] = atomicAdd(&cnt[ci[i]], 1u);
    }
    asm volatile("s_waitcnt vmcnt(8)" : "+v"(ep) :: "memory");
    if (MODE == 0 && t == 447) *(u64*)(lds + L_MISC + 32) = (ep + 1) & 0xffffffffffull;
    __syncthreads();
    u32x4 ca, cb; u32 tot = 0, incl = 0;
    if (MODE == 0) tag = *(const u64*)(lds + L_MISC + 32);
    if (t < NWG) {
      ca = *(const u32x4*)&cnt[8 * t]; cb = *(const u32x4*)&cnt[8 * t + 4];
      u32 run = 0, v;
      #pragma unroll
      for (int i = 0; i < 4; ++i) { v = ca[i]; ca[i] = run; run += v; }
      #pragma unroll
      for (int i = 0; i < 4; ++i) { v = cb[i]; cb[i] = run; run += v; }
      tot = run; incl = wave_incl_scan(tot);
      if (lane == 63) wtot[wv] = incl;
    }
    __syncthreads();
    if (t < NWG) {
      const u32x4 wt = *(const u32x4*)wtot;
      const u32 base = incl - tot + (wv > 0 ? wt[0] : 0) + (wv > 1 ? wt[1] : 0) + (wv > 2 ? wt[2] : 0);
      *(u32x4*)&cnt[8 * t] = ca + base; *(u32x4*)&cnt[8 * t + 4] = cb + base;
      const u32 gval = base | ((base + tot) << 12);
      GASP(u64) g = (GASP(u64))(ws + W_GRAN) + (size_t)t * NWG + p;
      if (MODE == 0) __hip_atomic_store(g, (tag << 24) | gval, __ATOMIC_RELAXED, __HIP_MEMORY_SCOPE_AGENT);
      else *g = gval;
    }
    if (MODE == 0)
      asm volatile("s_cmp_lt_u32 %7, 4\n\ts_cbranch_scc1 wlo_%=\n\ts_waitcnt vmcnt(1)\nwlo_%=:\n\ts_waitcnt vmcnt(2)"
                   : "+v"(sv[0]), "+v"(sv[1]), "+v"(av), "+v"(rv[0]), "+v"(rv[1]), "+v"(rv[2]), "+v"(epsv) : "s"(wv) : "scc", "memory");
    else
      asm volatile("s_waitcnt vmcnt(0)" : "+v"(sv[0]), "+v"(sv[1]), "+v"(av), "+v"(rv[0]), "+v"(rv[1]), "+v"(rv[2]), "+v"(epsv), "+v"(mpf) :: "memory");
    u32 c01[8], c23[8], kk[8];
    #pragma unroll
    for (int i = 0; i < 8; ++i) {
      const u32 aw = av[i >> 1]; const float a = (i & 1) ? bfhi(aw) : bf2f(aw);
      float y[3];
      #pragma unroll
      for (int j = 0; j < 3; ++j) { const int id = 3 * i + j; const u32 w = rv[id >> 3][(id >> 1) & 3]; y[j] = (id & 1) ? bfhi(w) : bf2f(w); }
      c01[i] = pk2(a, a * y[0]); c23[i] = pk2(a * y[1], a * y[2]);
      kk[i] = (sv[i >> 2][i & 3] << 9) | nl[i];
      opaque(c01[i]); opaque(c23[i]); opaque(kk[i]);
    }
    __syncthreads();
    const u32 tag32 = (u32)tag, tagk = MODE == 0 ? (((u32)tag & 0x1ffu) << 23) | ((((u32)tag >> 9) & 7u) << 6) : 0u;
    #pragma unroll
    for (int i = 0; i < 8; ++i) rk[i] += cnt[ci[i]];
    #pragma unroll
    for (int i = 0; i < 8; ++i) {
      if (i < nmine) srec[rk[i]] = u32x4{kk[i] | tagk, c01[i], c23[i], tag32};
    }
    if (MODE == 0) clear_small();
    __syncthreads();
    if (MODE == 0) {
      const u64* gp = (const u64*)(ws + W_GRAN) + (size_t)(xcd * 32 + rank) * NWG + 32 * wv + (lane & 31);
      asm volatile("global_load_dwordx2 %0, %1, off sc1" : "=&v"(gv0) : "v"(gp) : "memory");
    }
    {
      u32x4 v[7];
      #pragma unroll
      for (int i = 0; i < 7; ++i) { const u32 o = t + 512 * i; v[i] = srec[o < CHMAX ? o : 0]; }
      #pragma unroll
      for (int i = 0; i < 7; ++i) {
        const u32 o = t + 512 * i;
        __builtin_amdgcn_raw_buffer_store_b128(v[i], rrec, (int)o < nval ? (p * CH + o) * 16 : OOB, 0, AUX);
      }
    }
  }
  if (MODE == 1) return;


  const int bkt = xcd * 32 + rank;
  const int node0 = bkt * NB;
  const int nn = min(NB, N - node0);
  u32* keys = (u32*)(lds + LB_KEY); u32x2* pay = (u32x2*)(lds + LB_PAY);
  u32* mark = (u32*)(lds + LB_MARK) + wv * 16;
  u32* ncnt = (u32*)(lds + LB_NCNT); u32* cntb = (u32*)(lds + LB_CNT); u32* tab = (u32*)(lds + LB_TAB) + wv * 128;
  u32* misc = (u32*)(lds + L_MISC);

  weight_dma();
  scale_loads();
  f32x16 acc[8];
  #pragma unroll
  for (int j = 0; j < 8; ++j)
    #pragma unroll
    for (int r = 0; r < 16; ++r) acc[j][r] = 0.f;
  const int h = lane >> 5, m = lane & 31;
  const __amdgpu_buffer_rsrc_t rs = RSRC(x, N * 512);
  const u32 moff = m * 16;
  const u32 rot = 2 * xcd;
  const int sup_r = (lane >> 2) & 3;
  const u32 myslot = 4 * ((lane >> 4) & 1) + (lane & 3);
  u32* ninf = (u32*)(lds + L_NINF); u32* fnl = (u32*)(lds + L_FN);
  if (MODE == 2) { clear_small(); __syncthreads(); }
  opaque(epsv);
  if (t < 36) {
    const int qq = t / 9, jk = t - 9 * qq;
    ((float*)(lds + L_KAP))[qq * 12 + jk] = qq == 0 ? ((jk == 0 || jk == 4 || jk == 8) ? 1.f : 0.f) : bf2f(epsv);
  }
  if (wv == 0) {
    const int e_ = t - 9, ii = e_ / 9, jj = (e_ / 3) % 3, kk_ = e_ % 3;
    const bool dense = t >= 9 && t < 36 && (ii == jj || jj == kk_ || ii == kk_) && (epsv & 0x7fff) != 0;
    const u32 any = __any(dense);
    if (t == 0) { misc[10] = any; *(u32x4*)(lds + L_ZERO) = u32x4{0, 0, 0, 0}; }
  }
  asm volatile("" : "+v"(acc[0]), "+v"(acc[1]), "+v"(acc[2]), "+v"(acc[3]), "+v"(acc[4]), "+v"(acc[5]), "+v"(acc[6]), "+v"(acc[7]));
  __builtin_amdgcn_sched_barrier(0);

  const int sp = 32 * wv + (lane & 31);
  u32 o;
  {
    GASP(u64) g = (GASP(u64))(ws + W_GRAN) + (size_t)bkt * NWG + sp;
    if (MODE == 0) {
      asm volatile("s_waitcnt vmcnt(17)" : "+v"(gv0), "+v"(mpf) :: "memory");
      u32 vlo = (u32)gv0;
      if (__builtin_expect(!__all((gv0 >> 24) == tag), 0)) {
        u64 v; u32 spins = 0;
        do {
          v = __hip_atomic_load(g, __ATOMIC_RELAXED, __HIP_MEMORY_SCOPE_AGENT);
        } while (!__all((v >> 24) == tag) && ++spins < (1u << 17));
        vlo = ((v >> 24) == tag) ? (u32)v : 0u;
        opaque(vlo);
      }
      o = vlo & 0xffffffu;
    } else o = (u32)*g & 0xffffffu;
  }
  const u32 sstart = o & 0xfff;
  const u32 slen = (lane < 32) ? (o >> 12) - sstart : 0u;
  const u32 sincl = wave_incl_scan(slen);
  const u32 spre = sincl - slen;
  u32 cntw = __builtin_amdgcn_readlane(sincl, 63);
  const u64 nem = __ballot(slen != 0);
  const u32 nrk = __builtin_amdgcn_mbcnt_hi((u32)(nem >> 32), __builtin_amdgcn_mbcnt_lo((u32)nem, 0));
  const u32 sG = (u32)__builtin_amdgcn_ds_permute((int)((slen != 0 ? nrk : 63u) << 2), (int)((u32)sp * CH + sstart - spre));
  if (lane == 0) misc[wv] = cntw;

  const u32 tag32b = (u32)tag, tagkb = (((u32)tag & 0x1ffu) << 23) | ((((u32)tag >> 9) & 7u) << 6);
  u32 maxw = 0;
  auto round = [&](const u32 r0, auto first_tag) {
    constexpr bool FIRST = decltype(first_tag)::value;
    { const u32 pb = spre - 1 - r0;
      if (slen != 0 && spre != 0 && pb < 512) atomicOr(&mark[pb >> 5], 1u << (pb & 31)); }
    u32 carry = __builtin_popcountll(__ballot(slen != 0 && spre != 0 && spre <= r0));
    u32x4 mk[4];
    #pragma unroll
    for (int i = 0; i < 4; ++i) mk[i] = *(const u32x4*)&mark[4 * i];
    u32 gi[8];
    #pragma unroll
    for (int i = 0; i < 8; ++i) {
      const u32 lo = mk[i >> 1][2 * (i & 1)], hi = mk[i >> 1][2 * (i & 1) + 1];
      const u32 rkk = __builtin_amdgcn_mbcnt_hi(hi, __builtin_amdgcn_mbcnt_lo(lo, carry));
      carry += __builtin_popcount(lo) + __builtin_popcount(hi);
      gi[i] = (u32)__builtin_amdgcn_ds_bpermute((int)(rkk << 2), (int)sG);
    }
    __builtin_amdgcn_sched_barrier(0);
    u32 k[8], br[8]; u32x2 py[8];
    #pragma unroll
    for (int i = 0; i < 8; ++i) {
      const u32 r = r0 + lane + 64 * i;
      u32 g = (gi[i] + r) * 16; opaque(g);
      gi[i] = (r < cntw) ? g : OOB;
    }
    u32x4 v[8];
    #pragma unroll
    for (int i = 0; i < 8; ++i) v[i] = __builtin_amdgcn_raw_buffer_load_b128(rrec, gi[i], 0, AUX);
    __builtin_amdgcn_sched_barrier(0);
    if (FIRST) {
      asm volatile("s_waitcnt vmcnt(8)" : "+v"(wsc5[0]), "+v"(wsc5[1]), "+v"(wsc5[2]), "+v"(wsc5[3]), "+v"(wsc5[4]) :: "memory");
      scale_weights();
    }
    __builtin_amdgcn_sched_barrier(0);
    for (u32 spins = 0;;) {
      u32 bad = 0;
      #pragma unroll
      for (int i = 0; i < 8; ++i)
        if (MODE == 0) bad |= (gi[i] != OOB) ? ((v[i][3] ^ tag32b) | ((v[i][0] ^ tagkb) & ~KEYMASK)) : 0u;
      if (MODE != 0 || !__any(bad != 0)) break;
      if (++spins > (1u << 16)) { cntw = 0; break; }
      __builtin_amdgcn_s_sleep(1);
      asm volatile("" ::: "memory");
      #pragma unroll
      for (int i = 0; i < 8; ++i) v[i] = __builtin_amdgcn_raw_buffer_load_b128(rrec, gi[i], 0, AUX);
    }
    #pragma unroll
    for (int i = 0; i < 8; ++i) { k[i] = MODE == 0 ? v[i][0] & KEYMASK : v[i][0]; py[i] = u32x2{v[i][1], v[i][2]}; }
    u32 rk1[8], rng[8];
    #pragma unroll
    for (int i = 0; i < 8; ++i) {
      u32 ix = wv * 64 + (k[i] & 63); opaque(ix);
      ix = (r0 + lane + 64 * i < cntw) ? ix : 512 + lane;
      rk1[i] = atomicAdd(&ncnt[ix], 1u);
    }
    #pragma unroll
    for (int i = 0; i < 8; ++i) { rng[i] = ((mul24s(k[i] >> 9, rmul) >> 16) - rot) & (NR - 1); opaque(rng[i]); }
    __syncthreads();
    { const u32x4 m0 = *(const u32x4*)misc, m1 = *(const u32x4*)(misc + 4);
      maxw = __builtin_amdgcn_readfirstlane(max(max(max(m0[0], m0[1]), max(m0[2], m0[3])), max(max(m1[0], m1[1]), max(m1[2], m1[3])))); }
    if (FIRST && MODE == 0 && blockIdx.x == 0 && t == 0) {
      __hip_atomic_store((GASP(u64))(ws + W_EPOCH), tag, __ATOMIC_RELAXED, __HIP_MEMORY_SCOPE_AGENT);
    }
    bool split = maxw <= 512;
    u32 cw[8]; u32 fnv = 0, ninfo = 0;
    if (split) {
      u32 c[8];
      #pragma unroll
      for (int w = 0; w < 8; ++w) c[w] = ncnt[w * 64 + lane];
      u32 tot = 0, pre = 0;
      #pragma unroll
      for (int w = 0; w < 8; ++w) { pre = (w == wv) ? tot : pre; tot += c[w]; }
      const u32 S = wave_incl_scan(tot);
      const u32 ns = S - tot, nb = ns + pre;
      const u32 total = __builtin_amdgcn_readlane(S, 63);
      const u32 T = (total + 15) >> 4;
      const u32 bs = max(T >> 3, 1u), n1 = 8 - (T > 8 * bs ? T - 8 * bs : 0u);
      const u32 M1 = __builtin_amdgcn_readfirstlane((u32)(65536.f * __builtin_amdgcn_rcpf((float)bs)) + 2);
      const u32 M2 = __builtin_amdgcn_readfirstlane((u32)(65536.f * __builtin_amdgcn_rcpf((float)(bs + 1))) + 2);
      auto wave_of = [&](u32 x) { const u32 u = x >> 4; return min(mul24s(u, M1) >> 16, mul24s(u + n1, M2) >> 16); };
      auto start_of = [&](u32 g) { return (mul24s(g, bs) + (g > n1 ? g - n1 : 0u)) << 4; };
      const u32 wf = wave_of(ns);
      const u32 wl = wave_of(S - 1);
      const u32 wfL = start_of(wf);
      const u32 gl = (wfL == ns) ? wf : wf + 1;
      const bool has = (tot != 0) & (start_of(gl) < S);
      fnv = (u32)__builtin_amdgcn_ds_permute((int)((has ? gl : 63u) << 2), (int)lane);
      const u32 S8 = (u32)__builtin_amdgcn_ds_bpermute((int)((lane - 8) << 2), (int)S);
      #pragma unroll
      for (int i = 0; i < 8; ++i) rk1[i] += (u32)__builtin_amdgcn_ds_bpermute((int)((k[i] & 63) << 2), (int)nb);
      __builtin_amdgcn_sched_barrier(0);
      fnv = (lane < 8 && start_of(lane) < total) ? fnv : 64u;
      split = !__any((tot != 0) & ((wl > gl) | ((lane >= 8) & (S8 > wfL))));
      ninfo = tot ? (wf | ((wl - wf + 1) << 4)) : 0u;
      #pragma unroll
      for (int i = 0; i < 8; ++i) cw[i] = wave_of(rk1[i]);
    }
    if (!split) {
      fnv = 7 * lane; ninfo = ((lane * 37) >> 8) | 16u;
      #pragma unroll
      for (int i = 0; i < 8; ++i) cw[i] = ((k[i] & 63) * 37) >> 8;
    }
    if (wv == 0) { ninf[lane] = ninfo; if (lane < 8) fnl[lane] = fnv; }
    const u32 myfn = __builtin_amdgcn_readlane(fnv, wv);
    u32 bin[8];
    #pragma unroll
    for (int i = 0; i < 8; ++i) {
      bin[i] = cw[i] * NR + rng[i];
      opaque(bin[i]);
      bin[i] = (r0 + lane + 64 * i < cntw) ? bin[i] : 1024 + lane - wv * 128;
    }
    __builtin_amdgcn_sched_barrier(0);
    #pragma unroll
    for (int i = 0; i < 8; ++i) br[i] = atomicAdd(&cntb[wv * 128 + bin[i]], 1u);
    __builtin_amdgcn_sched_barrier(0);
    __syncthreads();
    u32 mylb, mycnt;
    {
      u32x2 c[8];
      #pragma unroll
      for (int w = 0; w < 8; ++w) c[w] = *(const u32x2*)&cntb[w * 128 + 2 * lane];
      u32 t0 = 0, t1 = 0, p0 = 0, p1 = 0;
      #pragma unroll
      for (int w = 0; w < 8; ++w) { p0 = (w == wv) ? t0 : p0; p1 = (w == wv) ? t1 : p1; t0 += c[w][0]; t1 += c[w][1]; }
      const u32 ps = t0 + t1;
      const u32 S = wave_incl_scan(ps);
      const u32 send = bcast8<7>(S), gstart = bcast8<0>(S - ps);
      const u32 len = send - gstart;
      const u32 padl = ((lane & 7) == 7) ? ((len + 15) & ~15u) + 16 - len : 0u;
      const u32 padprev = bcast8<0>(wave_incl_scan(padl));
      const u32 b0 = S - ps + padprev;
      *(u32x2*)&tab[2 * lane] = u32x2{b0 + p0, b0 + t0 + p1};
      mylb = __builtin_amdgcn_readlane(gstart + padprev, 8 * wv);
      mycnt = __builtin_amdgcn_readlane(len, 8 * wv);
    }
    #pragma unroll
    for (int i = 0; i < 8; ++i) br[i] += tab[bin[i] & 127];
    #pragma unroll
    for (int i = 0; i < 8; ++i) {
      u32 pos = br[i]; opaque(pos);
      pos = (r0 + lane + 64 * i < cntw) ? pos : NSLOT - 64 + lane;
      keys[pos] = k[i]; ((u32*)pay)[2 * pos] = py[i][0]; ((u32*)pay)[2 * pos + 1] = py[i][1];
    }
    const u32 nsteps = (mycnt + 15) >> 4;
    if (lane < 32) { const u32 i = mycnt + lane; if (i < nsteps * 16 + 16) { keys[mylb + i] = PADKEY; pay[mylb + i] = u32x2{0, 0}; } }
    __syncthreads();

    {
      const u32 mynode = myfn + myslot;
      const u32* kp = keys + mylb + 8 * h;
      const u32* ks = keys + mylb + 8 * h + sup_r;
      const u32 payaddr = ldsbase + LB_PAY + (mylb + 8 * h + sup_r) * 8;
      const u32 zaddr = ldsbase + L_ZERO;
      u32x4 R[8];
      {
        const u32x4 ka = *(const u32x4*)kp, kb = *(const u32x4*)(kp + 4);
        #pragma unroll
        for (int i = 0; i < 4; ++i) R[i] = __builtin_amdgcn_raw_buffer_load_b128(rs, (ka[i] & ~511u) | moff, 0, 0);
        #pragma unroll
        for (int i = 0; i < 4; ++i) R[4 + i] = __builtin_amdgcn_raw_buffer_load_b128(rs, (kb[i] & ~511u) | moff, 0, 0);
      }
      for (u32 s = 0; s < nsteps; ++s) {
        if (((s + (wv >> 2)) & 1) != 0) __builtin_amdgcn_s_setprio(1); else __builtin_amdgcn_s_setprio(0);
        const u32x4 ka = *(const u32x4*)(kp + 16 * (s + 1)), kb = *(const u32x4*)(kp + 16 * (s + 1) + 4);
        const u32 k0 = ks[16 * s], k1 = ks[16 * s + 4];
        const u32 a0 = ((k0 & 63) == mynode) ? payaddr + s * 128 : zaddr;
        const u32 a1 = ((k1 & 63) == mynode) ? payaddr + s * 128 + 32 : zaddr;
        const s16x4 c0 = __builtin_amdgcn_ds_read_tr16_b64_v4i16((LDSP(s16x4))(uintptr_t)a0);
        const s16x4 c1 = __builtin_amdgcn_ds_read_tr16_b64_v4i16((LDSP(s16x4))(uintptr_t)a1);
        const bf16x8 cf = __builtin_bit_cast(bf16x8, __builtin_shufflevector(c0, c1, 0, 1, 2, 3, 4, 5, 6, 7));
        u32x4 F[8];
        #pragma unroll
        for (int dd = 0; dd < 4; ++dd) {
          #pragma unroll
          for (int j = 0; j < 8; ++j)
            F[j][dd] = __builtin_amdgcn_perm(R[2 * dd + 1][j >> 1], R[2 * dd][j >> 1], (j & 1) ? 0x07060302u : 0x05040100u);
          __builtin_amdgcn_sched_barrier(0);
          const u32 ke = dd < 2 ? ka[2 * dd] : kb[2 * dd - 4], ko = dd < 2 ? ka[2 * dd + 1] : kb[2 * dd - 3];
          R[2 * dd] = __builtin_amdgcn_raw_buffer_load_b128(rs, (ke & ~511u) | moff, 0, 0);
          R[2 * dd + 1] = __builtin_amdgcn_raw_buffer_load_b128(rs, (ko & ~511u) | moff, 0, 0);
          __builtin_amdgcn_sched_barrier(0);
        }
        #pragma unroll
        for (int j = 0; j < 8; ++j) acc[j] = __builtin_amdgcn_mfma_f32_32x32x16_bf16(__builtin_bit_cast(bf16x8, F[j]), cf, acc[j], 0, 0, 0);
        __builtin_amdgcn_sched_barrier(0);
      }
    }
  };
  round(0u, std::true_type{});
  for (u32 r0 = 512; r0 < maxw; r0 += 512) {
    __syncthreads(); clear_small(); __syncthreads();
    round(r0, std::false_type{});
  }
  __builtin_amdgcn_s_setprio(0);

  u32 te = t; opaque(te);
  const int q = te & 3;
  const float dl1 = q == 1 ? 1.f : 0.f, dl2 = q == 2 ? 1.f : 0.f, dl3 = q == 3 ? 1.f : 0.f;
  f32x16 D1[2], D2[2];
  const u32 he = (te >> 5) & 1, me = te & 31;
  const u32 wrow = 32 * he + ((te >> 2) & 3);
  const u32 wa0 = ldsbase + L_W + wrow * 128 + 32 * ((te >> 4) & 1) + 8 * (te & 3) + ((wrow >> 1) & 1) * 64;
  const u32 wa1 = wa0 ^ 64;
  auto wfrag = [&](int g) {
    const int a = g / 10, tau = (g % 10) / 5, kind = g % 5;
    const u32 aa = (tau ? wa1 : wa0) + (64 * kind + 8 * a) * 128;
    const s16x4 lo = __builtin_amdgcn_ds_read_tr16_b64_v4i16((LDSP(s16x4))(uintptr_t)aa);
    const s16x4 hi = __builtin_amdgcn_ds_read_tr16_b64_v4i16((LDSP(s16x4))(uintptr_t)(aa + 512));
    return __builtin_bit_cast(bf16x8, __builtin_shufflevector(lo, hi, 0, 1, 2, 3, 4, 5, 6, 7));
  };
  const u32 dense = __builtin_amdgcn_readfirstlane(((const u32*)(lds + L_MISC))[10]);
  float kap[9];
  { const float* kq = (const float*)(lds + L_KAP) + q * 12;
    const f32x4 k0 = *(const f32x4*)kq, k1 = *(const f32x4*)(kq + 4); const float k8 = kq[8];
    const float a_ = q == 1 ? 0.f : q == 2 ? k1[2] : q == 3 ? k0[3] : k0[0];
    const float b_ = q == 2 ? 0.f : q == 1 ? k1[3] : q == 3 ? k0[1] : k1[0];
    const float c_ = q == 3 ? 0.f : q == 1 ? k1[1] : q == 2 ? k0[2] : k8;
    kap[0] = dense ? k0[0] : a_; kap[1] = dense ? k0[1] : b_; kap[2] = dense ? k0[2] : c_;
    kap[3] = k0[3]; kap[4] = k1[0]; kap[5] = k1[1]; kap[6] = k1[2]; kap[7] = k1[3]; kap[8] = k8; }
  asm volatile("s_nop 15\n\ts_nop 15" : "+v"(acc[0]), "+v"(acc[1]), "+v"(acc[2]), "+v"(acc[3]), "+v"(acc[4]), "+v"(acc[5]), "+v"(acc[6]), "+v"(acc[7]));
  auto mix2 = [&](int a, int jp, u32x4& Xa, u32x4& Xb, u32x4& Xc) {
    const int j = 2 * jp;
    const float t1 = acc[j][4 + a], t2 = acc[j][8 + a], t3 = acc[j][12 + a];
    const float u1 = acc[j + 1][4 + a], u2 = acc[j + 1][8 + a], u3 = acc[j + 1][12 + a];
    float b0, c0, b1, c1;
    #define QP(n) " quad_perm:[" #n "," #n "," #n "," #n "] row_mask:0xf bank_mask:0xf\n\t"
    #define QQ(a_, b_, c_, d_) " quad_perm:[" #a_ "," #b_ "," #c_ "," #d_ "] row_mask:0xf bank_mask:0xf\n\t"
    asm("s_cmp_lg_u32 %22, 0\n\ts_cbranch_scc1 mixd_%=\n\t"
        "v_mul_f32_dpp %1, %4, %13" QQ(1, 1, 3, 2) "v_mul_f32_dpp %3, %7, %13" QQ(1, 1, 3, 2)
        "v_fmac_f32_dpp %1, %5, %14" QQ(2, 3, 2, 1) "v_fmac_f32_dpp %3, %8, %14" QQ(2, 3, 2, 1)
        "v_fmac_f32_dpp %1, %6, %15" QQ(3, 2, 1, 3) "v_fmac_f32_dpp %3, %9, %15" QQ(3, 2, 1, 3)
        "mixe_%=:\n\t"
        "v_mul_f32_dpp %0, %4, %10" QP(0) "v_mul_f32_dpp %2, %7, %10" QP(0)
        "v_fmac_f32_dpp %0, %5, %11" QP(0) "v_fmac_f32_dpp %2, %8, %11" QP(0)
        "v_fmac_f32_dpp %0, %6, %12" QP(0) "v_fmac_f32_dpp %2, %9, %12" QP(0)
        ".subsection 1\n"
        "mixd_%=:\n\t"
        "v_mul_f32_dpp %1, %4, %13" QP(1) "v_mul_f32_dpp %3, %7, %13" QP(1)
        "v_fmac_f32_dpp %1, %5, %14" QP(1) "v_fmac_f32_dpp %3, %8, %14" QP(1)
        "v_fmac_f32_dpp %1, %6, %15" QP(1) "v_fmac_f32_dpp %3, %9, %15" QP(1)
        "v_fmac_f32_dpp %1, %4, %16" QP(2) "v_fmac_f32_dpp %3, %7, %16" QP(2)
        "v_fmac_f32_dpp %1, %5, %17" QP(2) "v_fmac_f32_dpp %3, %8, %17" QP(2)
        "v_fmac_f32_dpp %1, %6, %18" QP(2) "v_fmac_f32_dpp %3, %9, %18" QP(2)
        "v_fmac_f32_dpp %1, %4, %19" QP(3) "v_fmac_f32_dpp %3, %7, %19" QP(3)
        "v_fmac_f32_dpp %1, %5, %20" QP(3) "v_fmac_f32_dpp %3, %8, %20" QP(3)
        "v_fmac_f32_dpp %1, %6, %21" QP(3) "v_fmac_f32_dpp %3, %9, %21" QP(3)
        "s_branch mixe_%=\n"
        ".subsection 0"
        : "=&v"(b0), "=&v"(c0), "=&v"(b1), "=&v"(c1)
        : "v"(t1), "v"(t2), "v"(t3), "v"(u1), "v"(u2), "v"(u3), "v"(dl1), "v"(dl2), "v"(dl3),
          "v"(kap[0]), "v"(kap[1]), "v"(kap[2]), "v"(kap[3]), "v"(kap[4]), "v"(kap[5]), "v"(kap[6]), "v"(kap[7]), "v"(kap[8]), "s"(dense)
        : "scc");
    #undef QP
    #undef QQ
    Xa[jp] = pk2(acc[j][a], acc[j + 1][a]); Xb[jp] = pk2(b0, b1); Xc[jp] = pk2(c0, c1);
  };
  u32x4 X[2][3];
  bf16x8 wf[3];
  wf[0] = wfrag(0); wf[1] = wfrag(1);
  #pragma unroll
  for (int jp = 0; jp < 4; ++jp) mix2(0, jp, X[0][0], X[0][1], X[0][2]);
  #pragma unroll
  for (int g = 0; g < 40; ++g) {
    const int a = g / 10, i = g % 10, tau = i / 5, kind = i % 5;
    __builtin_amdgcn_sched_barrier(0);
    if (g + 2 < 40) wf[(g + 2) % 3] = wfrag(g + 2);
    const bf16x8 xb = __builtin_bit_cast(bf16x8, X[a & 1][kind == 1 ? 1 : (kind == 2 || kind == 4) ? 2 : 0]);
    const f32x16 zero16 = {0, 0, 0, 0, 0, 0, 0, 0, 0, 0, 0, 0, 0, 0, 0, 0};
    if (kind < 3) D1[tau] = __builtin_amdgcn_mfma_f32_32x32x16_bf16(wf[g % 3], xb, (a == 0 && kind == 0) ? zero16 : D1[tau], 0, 0, 0);
    else D2[tau] = __builtin_amdgcn_mfma_f32_32x32x16_bf16(wf[g % 3], xb, (a == 0 && kind == 3) ? zero16 : D2[tau], 0, 0, 0);
    if (a < 3 && i < 8 && (i & 1) == 0) mix2(a + 1, i >> 1, X[(a + 1) & 1][0], X[(a + 1) & 1][1], X[(a + 1) & 1][2]);
  }
  __builtin_amdgcn_sched_barrier(0);
  const int v = te >> 3, k8 = te & 7;
  const u32 inf = ((const u32*)(lds + L_NINF))[v & 63];
  const u32 w0_ = inf & 15, nw = inf >> 4;
  const u32 sl0 = v - ((const u32*)(lds + L_FN))[w0_ & 7];
  const float* ob0 = (const float*)(lds + LE_OUT) + ((w0_ * 32 + 4 * sl0) & 255) * 68 + 8 * k8;
  asm volatile("" :: "v"(ob0), "v"(nw));
  __syncthreads();
  {
    float* ob = (float*)(lds + LE_OUT) + (wv * 32 + me) * 68 + 4 * he;
    #pragma unroll
    for (int tau = 0; tau < 2; ++tau)
      #pragma unroll
      for (int g4 = 0; g4 < 4; ++g4) {
        f32x4 v;
        #pragma unroll
        for (int u = 0; u < 4; ++u) v[u] = (q == 0) ? D2[tau][4 * g4 + u] : D1[tau][4 * g4 + u];
        *(f32x4*)(ob + 32 * tau + 8 * g4) = v;
      }
  }
  __syncthreads();
  if ((int)te < nn * 8) {
    f32x4 u[4][2];
    { const float* ob = ob0;
      #pragma unroll
      for (int i = 0; i < 4; ++i) { u[i][0] = *(const f32x4*)(ob + 68 * i); u[i][1] = *(const f32x4*)(ob + 68 * i + 4); }
      if (nw == 0) {
        #pragma unroll
        for (int i = 0; i < 4; ++i) { u[i][0] = f32x4{0, 0, 0, 0}; u[i][1] = f32x4{0, 0, 0, 0}; }
      } }
    #pragma unroll 1
    for (u32 c = 1; c < nw; ++c) {
      const float* ob = (const float*)(lds + LE_OUT) + ((w0_ + c) * 32) * 68 + 8 * k8;
      #pragma unroll
      for (int i = 0; i < 4; ++i) { u[i][0] += *(const f32x4*)(ob + 68 * i); u[i][1] += *(const f32x4*)(ob + 68 * i + 4); }
    }
    float gate[8];
    #pragma unroll
    for (int i = 0; i < 4; ++i) {
      gate[i] = __builtin_amdgcn_rcpf(1.f + __builtin_amdgcn_exp2f(-1.44269504f * u[0][0][i]));
      gate[4 + i] = __builtin_amdgcn_rcpf(1.f + __builtin_amdgcn_exp2f(-1.44269504f * u[0][1][i]));
    }
    u16* op = out + (size_t)(node0 + v) * 256 + 8 * k8;
    auto ost = [&](u16* pp, u32x4 v) { __builtin_nontemporal_store(v, (u32x4*)pp); };
    ost(op, u32x4{pk2(u[0][0][0], u[0][0][1]), pk2(u[0][0][2], u[0][0][3]), pk2(u[0][1][0], u[0][1][1]), pk2(u[0][1][2], u[0][1][3])});
    #pragma unroll
    for (int i = 1; i < 4; ++i)
      ost(op + 64 * i, u32x4{pk2(u[i][0][0] * gate[0], u[i][0][1] * gate[1]), pk2(u[i][0][2] * gate[2], u[i][0][3] * gate[3]),
                             pk2(u[i][1][0] * gate[4], u[i][1][1] * gate[5]), pk2(u[i][1][2] * gate[6], u[i][1][3] * gate[7])});
  }
}

extern "C" void kernel_launch(void* const* d_in, const int* in_sizes, int n_in, void* d_out, int out_size, void* d_ws, size_t ws_size,
                              hipStream_t stream) {
  const u16* x = (const u16*)d_in[0]; const u16* rhat = (const u16*)d_in[1]; const u16* ea = (const u16*)d_in[2];
  const int* src = (const int*)d_in[3]; const int* dst = (const int*)d_in[4];
  const u16* wrad = (const u16*)d_in[5]; const u16* w0 = (const u16*)d_in[6]; const u16* w1 = (const u16*)d_in[7]; const u16* eps = (const u16*)d_in[8];
  const int N = in_sizes[0] / 256, E = in_sizes[3];
  const int CH = ((E + NWG - 1) / NWG + 7) & ~7;
  if (N <= 0 || N > NWG * NB || CH > CHMAX - 8 || ws_size < W_REC + (size_t)NWG * CHMAX * 16) return;
  const u32 rmul = (u32)(((u64)NR << 16) / (u32)N);
  char* ws = (char*)d_ws;
  int dev = 0, cus = 0;
  (void)hipGetDevice(&dev);
  (void)hipDeviceGetAttribute(&cus, hipDeviceAttributeMultiprocessorCount, dev);
  if (cus >= NWG) {
    (void)hipFuncSetAttribute((const void*)k_main<0>, hipFuncAttributeMaxDynamicSharedMemorySize, L_TOTAL);
    k_main<0><<<NWG, 512, L_TOTAL, stream>>>(x, rhat, ea, src, dst, wrad, w0, w1, eps, (u16*)d_out, ws, N, E, CH, rmul);
  } else {
    (void)hipFuncSetAttribute((const void*)k_main<1>, hipFuncAttributeMaxDynamicSharedMemorySize, L_TOTAL);
    (void)hipFuncSetAttribute((const void*)k_main<2>, hipFuncAttributeMaxDynamicSharedMemorySize, L_TOTAL);
    k_main<1><<<NWG, 512, L_TOTAL, stream>>>(x, rhat, ea, src, dst, wrad, w0, w1, eps, (u16*)d_out, ws, N, E, CH, rmul);
    k_main<2><<<NWG, 512, L_TOTAL, stream>>>(x, rhat, ea, src, dst, wrad, w0, w1, eps, (u16*)d_out, ws, N, E, CH, rmul);
  }
}
```
